```python
import torch

def _softmax(x, axis=-1):
    return torch.softmax(x, dim=axis)
def _gelu(x):
    return torch.nn.functional.gelu(x, approximate="tanh")
def _silu(x):
    return torch.nn.functional.silu(x)
def _sigmoid(x):
    return torch.sigmoid(x)
def _layernorm(x, axis=-1, eps=1e-5):
    m = x.mean(dim=axis, keepdim=True)
    v = x.var(dim=axis, keepdim=True, unbiased=False)
    return (x - m) / torch.sqrt(v + eps)
def _scatter_add(tgt, idx, src, axis=0):
    return tgt.clone().index_add_(axis, idx, src)
def _segment_sum(src, seg, n):
    out = torch.zeros((n,) + src.shape[1:], dtype=src.dtype)
    return out.index_add_(0, seg, src)
def _pad(x, pw):
    flat = tuple(v for pair in reversed(pw) for v in pair)
    return torch.nn.functional.pad(x, flat)

kernel_name = 'interleave_layers__spectral_gfnet__glu_gated_swiglu__prompt'

def setup_inputs():

    _w = (lambda a: (lambda t: t.to(torch.bfloat16) if t.is_floating_point() else t)(torch.as_tensor(a)))
    def _A_setup():
        B, S, D = 1, 2048, 640
        Df = D // 2 + 1
        return {
            "x": _w((lambda *s: torch.randn(*s, dtype=torch.bfloat16) * 0.05)(B, S, D)),
            "w_r": _w((lambda *s: torch.randn(*s, dtype=torch.bfloat16) * 0.4)(S, Df) * 0.02),
            "w_i": _w((lambda *s: torch.randn(*s, dtype=torch.bfloat16) * 0.4)(S, Df) * 0.02),
        }
    _A_in = _A_setup()

    def _B_setup():
        B, S, D, F = 1, 2048, 640, 1728
        return {
            "x": _w((lambda *s: torch.randn(*s, dtype=torch.bfloat16) * 0.05)(B, S, D)),
            "w1": _w((lambda *s: torch.randn(*s, dtype=torch.bfloat16) * 0.2)(D, F) * (2.0 / D) ** 0.5),
            "w3": _w((lambda *s: torch.randn(*s, dtype=torch.bfloat16) * 0.2)(D, F) * (2.0 / D) ** 0.5),
            "w2": _w((lambda *s: torch.randn(*s, dtype=torch.bfloat16) * 0.2)(F, D) * (2.0 / F) ** 0.5),
        }
    _B_in = _B_setup()

    return {
        "A_x": _A_in["x"],
        "A_w_r": _A_in["w_r"],
        "A_w_i": _A_in["w_i"],
        "B_x": _B_in["x"],
        "B_w1": _B_in["w1"],
        "B_w3": _B_in["w3"],
        "B_w2": _B_in["w2"],
    }

def reference(A_x, A_w_r, A_w_i, B_x, B_w1, B_w3, B_w2):
    def _A_ref(x, w_r, w_i):
        B, S, D = x.shape
        X = (lambda x, axes=(-2,-1): torch.fft.rfft2(x.float(), dim=axes))(x, axes=(-2, -1))
        W = w_r + 1j * w_i
        X = X * W
        y = (lambda x, s, axes=(-2,-1): torch.fft.irfft2(x, s=s, dim=axes).to(torch.bfloat16))(X, (S, D), axes=(-2, -1))
        return y

    def _B_ref(x, w1, w3, w2):
        gate = torch.einsum("bsd,df->bsf", x, w1)
        up = torch.einsum("bsd,df->bsf", x, w3)
        h = _silu(gate) * up
        out = torch.einsum("bsf,fd->bsd", h, w2)
        return out

    x = A_x
    x = x + _A_ref(_layernorm(x, axis=-1), A_w_r, A_w_i)
    x = x + _B_ref(_layernorm(x, axis=-1), B_w1, B_w3, B_w2)
    x = x + _A_ref(_layernorm(x, axis=-1), A_w_r, A_w_i)
    x = x + _B_ref(_layernorm(x, axis=-1), B_w1, B_w3, B_w2)
    return x
```

```cpp
#include <hip/hip_runtime.h>
#include <hip/hip_bf16.h>
#include <stdint.h>

typedef unsigned short u16;
using bf16x8 = __attribute__((ext_vector_type(8))) short;
using f32x4 = __attribute__((ext_vector_type(4))) float;

constexpr int S_ = 2048, D_ = 640, DF_ = 321, F_ = 1728;

__device__ __forceinline__ float bf2f(u16 v) { return __uint_as_float(((unsigned)v) << 16); }
__device__ __forceinline__ u16 f2bf(float f) { return __bfloat16_as_ushort(__float2bfloat16(f)); }
__device__ __forceinline__ float rbf(float f) { return bf2f(f2bf(f)); }

__device__ __forceinline__ float2 cadd(float2 a, float2 b) { return make_float2(a.x + b.x, a.y + b.y); }
__device__ __forceinline__ float2 csub(float2 a, float2 b) { return make_float2(a.x - b.x, a.y - b.y); }
__device__ __forceinline__ float2 cmul(float2 a, float2 b) {
  return make_float2(a.x * b.x - a.y * b.y, a.x * b.y + a.y * b.x);
}
template <int SG> __device__ __forceinline__ float2 mul_i(float2 a) {
  return SG > 0 ? make_float2(-a.y, a.x) : make_float2(a.y, -a.x);
}
template <int SG> __device__ __forceinline__ float2 twc(float2 w) {
  return SG > 0 ? make_float2(w.x, -w.y) : w;
}

template <int SG> __device__ __forceinline__ void dft4(float2& a0, float2& a1, float2& a2, float2& a3) {
  float2 s0 = cadd(a0, a2), d0 = csub(a0, a2), s1 = cadd(a1, a3), d1 = mul_i<SG>(csub(a1, a3));
  a0 = cadd(s0, s1); a2 = csub(s0, s1); a1 = cadd(d0, d1); a3 = csub(d0, d1);
}
template <int SG> __device__ __forceinline__ void dft8(float2* v) {
  float2 e0 = v[0], e1 = v[2], e2 = v[4], e3 = v[6], o0 = v[1], o1 = v[3], o2 = v[5], o3 = v[7];
  dft4<SG>(e0, e1, e2, e3); dft4<SG>(o0, o1, o2, o3);
  const float h = 0.70710678118654752440f;
  float2 t1 = make_float2(h * (o1.x - SG * o1.y), h * (o1.y + SG * o1.x));
  float2 t2 = mul_i<SG>(o2);
  float2 t3 = make_float2(h * (-o3.x - SG * o3.y), h * (-o3.y + SG * o3.x));
  v[0] = cadd(e0, o0); v[4] = csub(e0, o0);
  v[1] = cadd(e1, t1); v[5] = csub(e1, t1);
  v[2] = cadd(e2, t2); v[6] = csub(e2, t2);
  v[3] = cadd(e3, t3); v[7] = csub(e3, t3);
}
template <int SG> __device__ __forceinline__ void dft5(float2& a0, float2& a1, float2& a2, float2& a3, float2& a4) {
  const float c1 = 0.30901699437494742410f, c2 = -0.80901699437494742410f;
  const float s1 = 0.95105651629515357212f, s2 = 0.58778525229247312917f;
  float2 p1 = cadd(a1, a4), m1 = csub(a1, a4), p2 = cadd(a2, a3), m2 = csub(a2, a3);
  float2 t1 = make_float2(a0.x + c1 * p1.x + c2 * p2.x, a0.y + c1 * p1.y + c2 * p2.y);
  float2 t2 = make_float2(a0.x + c2 * p1.x + c1 * p2.x, a0.y + c2 * p1.y + c1 * p2.y);
  float2 u1 = mul_i<SG>(make_float2(s1 * m1.x + s2 * m2.x, s1 * m1.y + s2 * m2.y));
  float2 u2 = mul_i<SG>(make_float2(s2 * m1.x - s1 * m2.x, s2 * m1.y - s1 * m2.y));
  a0 = cadd(a0, cadd(p1, p2));
  a1 = cadd(t1, u1); a4 = csub(t1, u1);
  a2 = cadd(t2, u2); a3 = csub(t2, u2);
}
template <int SG> __device__ __forceinline__ void dft10(float2* v) {
  float2 e0 = v[0], e1 = v[2], e2 = v[4], e3 = v[6], e4 = v[8];
  float2 o0 = v[1], o1 = v[3], o2 = v[5], o3 = v[7], o4 = v[9];
  dft5<SG>(e0, e1, e2, e3, e4); dft5<SG>(o0, o1, o2, o3, o4);
  const float c1 = 0.80901699437494742410f, s1 = 0.58778525229247312917f;
  const float c2 = 0.30901699437494742410f, s2 = 0.95105651629515357212f;
  o1 = cmul(o1, make_float2(c1, SG * s1));
  o2 = cmul(o2, make_float2(c2, SG * s2));
  o3 = cmul(o3, make_float2(-c2, SG * s2));
  o4 = cmul(o4, make_float2(-c1, SG * s1));
  v[0] = cadd(e0, o0); v[5] = csub(e0, o0);
  v[1] = cadd(e1, o1); v[6] = csub(e1, o1);
  v[2] = cadd(e2, o2); v[7] = csub(e2, o2);
  v[3] = cadd(e3, o3); v[8] = csub(e3, o3);
  v[4] = cadd(e4, o4); v[9] = csub(e4, o4);
}

__device__ __forceinline__ int PD(int i) { return i + (i >> 3); }

template <int SG>
__device__ __forceinline__ void fft640(float2* b0, float2* b1, const float2* tw, int j) {
  float2 v[10];
#pragma unroll
  for (int r = 0; r < 8; ++r) v[r] = b0[PD(j + 80 * r)];
  dft8<SG>(v);
#pragma unroll
  for (int r = 0; r < 8; ++r) b1[PD(8 * j + r)] = v[r];
  __syncthreads();
  {
    int m = j & 7;
#pragma unroll
    for (int r = 0; r < 8; ++r) v[r] = b1[PD(j + 80 * r)];
#pragma unroll
    for (int r = 1; r < 8; ++r) v[r] = cmul(v[r], twc<SG>(tw[10 * r * m]));
    dft8<SG>(v);
#pragma unroll
    for (int r = 0; r < 8; ++r) b0[PD((j >> 3) * 64 + m + 8 * r)] = v[r];
  }
  __syncthreads();
  if (j < 64) {
#pragma unroll
    for (int r = 0; r < 10; ++r) v[r] = b0[PD(j + 64 * r)];
#pragma unroll
    for (int r = 1; r < 10; ++r) v[r] = cmul(v[r], twc<SG>(tw[r * j]));
    dft10<SG>(v);
#pragma unroll
    for (int r = 0; r < 10; ++r) b1[PD(j + 64 * r)] = v[r];
  }
}

__device__ __forceinline__ void ln16(float* xv, float* red, int row, int c) {
  float s = 0.f;
#pragma unroll
  for (int i = 0; i < 16; ++i) s += xv[i];
  s += __shfl_xor(s, 1); s += __shfl_xor(s, 2); s += __shfl_xor(s, 4);
  if ((c & 7) == 0) red[row * 5 + (c >> 3)] = s;
  __syncthreads();
  float mean = (red[row * 5] + red[row * 5 + 1] + red[row * 5 + 2] + red[row * 5 + 3] + red[row * 5 + 4]) * (1.0f / 640.0f);
  float q = 0.f;
#pragma unroll
  for (int i = 0; i < 16; ++i) { float d = xv[i] - mean; q += d * d; }
  q += __shfl_xor(q, 1); q += __shfl_xor(q, 2); q += __shfl_xor(q, 4);
  if ((c & 7) == 0) red[40 + row * 5 + (c >> 3)] = q;
  __syncthreads();
  float var = (red[40 + row * 5] + red[40 + row * 5 + 1] + red[40 + row * 5 + 2] + red[40 + row * 5 + 3] + red[40 + row * 5 + 4]) * (1.0f / 640.0f);
  float mb = rbf(mean), vb = rbf(var);
  float den = rbf(sqrtf(rbf(vb + 1e-5f)));
#pragma unroll
  for (int i = 0; i < 16; ++i) xv[i] = rbf(rbf(xv[i] - mb) / den);
}

constexpr int FB = 720;

__global__ __launch_bounds__(320) void k_a1(const u16* __restrict__ x, float2* __restrict__ Zt,
                                            const u16* __restrict__ wr, const u16* __restrict__ wi,
                                            unsigned* __restrict__ Wt, int do_prep) {
  __shared__ float2 buf[2][4][FB];
  __shared__ float2 tw[640];
  __shared__ float red[80];
  const int t = threadIdx.x, s0 = blockIdx.x * 8;
  if (do_prep) {
    for (int k = t; k < DF_; k += 320) {
      unsigned p[8];
#pragma unroll
      for (int r = 0; r < 8; ++r)
        p[r] = (unsigned)wr[(s0 + r) * DF_ + k] | ((unsigned)wi[(s0 + r) * DF_ + k] << 16);
      uint4* dst = (uint4*)(Wt + (size_t)k * S_ + s0);
      dst[0] = make_uint4(p[0], p[1], p[2], p[3]);
      dst[1] = make_uint4(p[4], p[5], p[6], p[7]);
    }
  }
  const int row = t / 40, c = t % 40;
  float xv[16];
  {
    const uint4* src = (const uint4*)(x + (size_t)(s0 + row) * D_ + c * 16);
    uint4 a = src[0], b = src[1];
    unsigned u[8] = {a.x, a.y, a.z, a.w, b.x, b.y, b.z, b.w};
#pragma unroll
    for (int i = 0; i < 8; ++i) { xv[2 * i] = __uint_as_float(u[i] << 16); xv[2 * i + 1] = __uint_as_float(u[i] & 0xffff0000u); }
  }
  for (int n = t; n < 640; n += 320) {
    float sn, cs; sincospif(-(float)n * (1.0f / 320.0f), &sn, &cs);
    tw[n] = make_float2(cs, sn);
  }
  ln16(xv, red, row, c);
  {
    float* dst = (float*)&buf[0][row >> 1][0] + (row & 1);
#pragma unroll
    for (int i = 0; i < 16; ++i) dst[2 * PD(c * 16 + i)] = xv[i];
  }
  __syncthreads();
  const int f = t / 80, j = t % 80;
  fft640<-1>(buf[0][f], buf[1][f], tw, j);
  __syncthreads();
  for (int e = t; e < DF_ * 4; e += 320) {
    int k = e >> 2, ff = e & 3;
    float2 a = buf[1][ff][PD(k)], b = buf[1][ff][PD(k == 0 ? 0 : 640 - k)];
    float4 o;
    o.x = 0.5f * (a.x + b.x); o.y = 0.5f * (a.y - b.y);
    o.z = 0.5f * (a.y + b.y); o.w = 0.5f * (b.x - a.x);
    *(float4*)(Zt + (size_t)k * S_ + s0 + 2 * ff) = o;
  }
}

__device__ __forceinline__ void transpose_tile(const u16* __restrict__ src, int ld_src, u16* __restrict__ dst, int ld_dst,
                                               int r0, int c0, int drow0, int dcol0, bool interleave, int il_off, u16* sm) {
  const int t = threadIdx.x;
  for (int i = 0; i < 2; ++i) {
    int e = t + i * 256;
    int r = e >> 3, cc = (e & 7) * 8;
    uint4 v = *(const uint4*)(src + (size_t)(r0 + r) * ld_src + c0 + cc);
    u16* p = (u16*)&v;
#pragma unroll
    for (int q = 0; q < 8; ++q) sm[(cc + q) * 66 + r] = p[q];
  }
  __syncthreads();
  for (int i = 0; i < 2; ++i) {
    int e = t + i * 256;
    int c = e >> 3, rr = (e & 7) * 8;
    uint4 v; u16* p = (u16*)&v;
#pragma unroll
    for (int q = 0; q < 8; ++q) p[q] = sm[c * 66 + rr + q];
    int col = c0 + c;
    int drow = interleave ? ((col >> 4) * 32 + il_off + (col & 15)) : col;
    *(uint4*)(dst + (size_t)drow * ld_dst + r0 + rr) = v;
  }
}

constexpr int CB = 2048 + 256;
__global__ __launch_bounds__(256) void k_a2(float2* __restrict__ Zt, const unsigned* __restrict__ Wt,
                                            const u16* __restrict__ w1, const u16* __restrict__ w3, const u16* __restrict__ w2,
                                            u16* __restrict__ w13p, u16* __restrict__ w2t) {
  __shared__ float2 smem[2 * CB + 2048];
  const int t = threadIdx.x;
  if (blockIdx.x >= DF_) {
    int id = blockIdx.x - DF_;
    int which = id / 270, tile = id % 270;
    u16* sm = (u16*)smem;
    if (which < 2) {
      int rt = tile / 27, ct = tile % 27;
      transpose_tile(which ? w3 : w1, F_, w13p, D_, rt * 64, ct * 64, 0, 0, true, which * 16, sm);
    } else {
      int rt = tile / 10, ct = tile % 10;
      transpose_tile(w2, D_, w2t, F_, rt * 64, ct * 64, 0, 0, false, 0, sm);
    }
    return;
  }
  float2* b0 = smem; float2* b1 = smem + CB; float2* tw = smem + 2 * CB;
  const int k = blockIdx.x;
  float2* col = Zt + (size_t)k * S_;
  float2 v[8];
#pragma unroll
  for (int r = 0; r < 8; ++r) v[r] = col[t + 256 * r];
  unsigned wv[8];
#pragma unroll
  for (int r = 0; r < 8; ++r) wv[r] = Wt[(size_t)k * S_ + t + 256 * r];
#pragma unroll
  for (int r = 0; r < 8; ++r) {
    int n = t + 256 * r; float sn, cs; sincospif(-(float)n * (1.0f / 1024.0f), &sn, &cs);
    tw[n] = make_float2(cs, sn);
  }
  dft8<-1>(v);
#pragma unroll
  for (int r = 0; r < 8; ++r) b0[PD(8 * t + r)] = v[r];
  __syncthreads();
  {
    int m = t & 7;
#pragma unroll
    for (int r = 0; r < 8; ++r) v[r] = b0[PD(t + 256 * r)];
#pragma unroll
    for (int r = 1; r < 8; ++r) v[r] = cmul(v[r], tw[32 * r * m]);
    dft8<-1>(v);
#pragma unroll
    for (int r = 0; r < 8; ++r) b1[PD((t >> 3) * 64 + m + 8 * r)] = v[r];
  }
  __syncthreads();
  {
    int m = t & 63;
#pragma unroll
    for (int r = 0; r < 8; ++r) v[r] = b1[PD(t + 256 * r)];
#pragma unroll
    for (int r = 1; r < 8; ++r) v[r] = cmul(v[r], tw[4 * r * m]);
    dft8<-1>(v);
#pragma unroll
    for (int r = 0; r < 8; ++r) b0[PD((t >> 6) * 512 + m + 64 * r)] = v[r];
  }
  __syncthreads();
#pragma unroll
  for (int h = 0; h < 2; ++h) {
    int j = t + 256 * h;
    float2 a0 = b0[PD(j)], a1 = b0[PD(j + 512)], a2 = b0[PD(j + 1024)], a3 = b0[PD(j + 1536)];
    a1 = cmul(a1, tw[j]); a2 = cmul(a2, tw[2 * j]); a3 = cmul(a3, tw[3 * j]);
    dft4<-1>(a0, a1, a2, a3);
    v[h] = a0; v[h + 2] = a1; v[h + 4] = a2; v[h + 6] = a3;
  }
  const float sc = 1.0f / (2048.0f * 640.0f);
#pragma unroll
  for (int r = 0; r < 8; ++r) {
    float2 w = make_float2(__uint_as_float(wv[r] << 16) * sc, __uint_as_float(wv[r] & 0xffff0000u) * sc);
    v[r] = cmul(v[r], w);
  }
  dft8<1>(v);
#pragma unroll
  for (int r = 0; r < 8; ++r) b1[PD(8 * t + r)] = v[r];
  __syncthreads();
  {
    int m = t & 7;
#pragma unroll
    for (int r = 0; r < 8; ++r) v[r] = b1[PD(t + 256 * r)];
#pragma unroll
    for (int r = 1; r < 8; ++r) v[r] = cmul(v[r], twc<1>(tw[32 * r * m]));
    dft8<1>(v);
#pragma unroll
    for (int r = 0; r < 8; ++r) b0[PD((t >> 3) * 64 + m + 8 * r)] = v[r];
  }
  __syncthreads();
  {
    int m = t & 63;
#pragma unroll
    for (int r = 0; r < 8; ++r) v[r] = b0[PD(t + 256 * r)];
#pragma unroll
    for (int r = 1; r < 8; ++r) v[r] = cmul(v[r], twc<1>(tw[4 * r * m]));
    dft8<1>(v);
#pragma unroll
    for (int r = 0; r < 8; ++r) b1[PD((t >> 6) * 512 + m + 64 * r)] = v[r];
  }
  __syncthreads();
#pragma unroll
  for (int h = 0; h < 2; ++h) {
    int j = t + 256 * h;
    float2 a0 = b1[PD(j)], a1 = b1[PD(j + 512)], a2 = b1[PD(j + 1024)], a3 = b1[PD(j + 1536)];
    a1 = cmul(a1, twc<1>(tw[j])); a2 = cmul(a2, twc<1>(tw[2 * j])); a3 = cmul(a3, twc<1>(tw[3 * j]));
    dft4<1>(a0, a1, a2, a3);
    col[j] = a0; col[j + 512] = a1; col[j + 1024] = a2; col[j + 1536] = a3;
  }
}

__global__ __launch_bounds__(320) void k_a3(const float2* __restrict__ Zt, const u16* xin,
                                            u16* xout, u16* __restrict__ xln) {
  __shared__ float2 buf[2][4][FB];
  __shared__ float2 tw[640];
  __shared__ float red[80];
  const int t = threadIdx.x, s0 = blockIdx.x * 8;
  for (int e = t; e < DF_ * 4; e += 320) {
    int k = e >> 2, ff = e & 3;
    float4 z = *(const float4*)(Zt + (size_t)k * S_ + s0 + 2 * ff);
    if (k == 0 || k == 320) {
      buf[0][ff][PD(k)] = make_float2(z.x, z.z);
    } else {
      buf[0][ff][PD(k)] = make_float2(z.x - z.w, z.y + z.z);
      buf[0][ff][PD(640 - k)] = make_float2(z.x + z.w, z.z - z.y);
    }
  }
  const int row = t / 40, c = t % 40;
  float xv[16];
  {
    const uint4* src = (const uint4*)(xin + (size_t)(s0 + row) * D_ + c * 16);
    uint4 a = src[0], b = src[1];
    unsigned u[8] = {a.x, a.y, a.z, a.w, b.x, b.y, b.z, b.w};
#pragma unroll
    for (int i = 0; i < 8; ++i) { xv[2 * i] = __uint_as_float(u[i] << 16); xv[2 * i + 1] = __uint_as_float(u[i] & 0xffff0000u); }
  }
  for (int n = t; n < 640; n += 320) {
    float sn, cs; sincospif(-(float)n * (1.0f / 320.0f), &sn, &cs);
    tw[n] = make_float2(cs, sn);
  }
  __syncthreads();
  const int f = t / 80, j = t % 80;
  fft640<1>(buf[0][f], buf[1][f], tw, j);
  __syncthreads();
  {
    const float* src = (const float*)&buf[1][row >> 1][0] + (row & 1);
    unsigned u[8];
#pragma unroll
    for (int i = 0; i < 16; ++i) xv[i] = rbf(xv[i] + rbf(src[2 * PD(c * 16 + i)]));
#pragma unroll
    for (int i = 0; i < 8; ++i) u[i] = (__float_as_uint(xv[2 * i]) >> 16) | (__float_as_uint(xv[2 * i + 1]) & 0xffff0000u);
    uint4* dst = (uint4*)(xout + (size_t)(s0 + row) * D_ + c * 16);
    dst[0] = make_uint4(u[0], u[1], u[2], u[3]); dst[1] = make_uint4(u[4], u[5], u[6], u[7]);
  }
  ln16(xv, red, row, c);
  {
    unsigned u[8];
#pragma unroll
    for (int i = 0; i < 8; ++i) u[i] = (__float_as_uint(xv[2 * i]) >> 16) | (__float_as_uint(xv[2 * i + 1]) & 0xffff0000u);
    uint4* dst = (uint4*)(xln + (size_t)(s0 + row) * D_ + c * 16);
    dst[0] = make_uint4(u[0], u[1], u[2], u[3]); dst[1] = make_uint4(u[4], u[5], u[6], u[7]);
  }
}

template <int BM, int BN, int EPI>
__global__ __launch_bounds__(256) void k_gemm(const u16* __restrict__ A, const u16* __restrict__ Bt, int K, int nbn,
                                              const u16* res, u16* out, int ldo) {
  constexpr int BK = 64;
  constexpr int TM = BM / 32, TN = BN / 32;
  constexpr int ABYTES = BM * BK * 2, BBYTES = BN * BK * 2;
  __shared__ __attribute__((aligned(16))) char lds[2 * (ABYTES + BBYTES)];
  const int t = threadIdx.x, wid = t >> 6, lane = t & 63, wr = wid >> 1, wc = wid & 1, fr = lane & 15, fq = lane >> 4;
  const int bm = blockIdx.x / nbn, bn = blockIdx.x % nbn;
  const int brow = bm * BM, bcol = bn * BN;
  auto stage = [&](int b, int kt) {
    char* la = lds + b * (ABYTES + BBYTES);
    char* lb = la + ABYTES;
#pragma unroll
    for (int i = 0; i < ABYTES / 4096; ++i) {
      int by = t * 16 + i * 4096, r = by >> 7, ch = ((by >> 4) & 7) ^ (r & 7);
      __builtin_amdgcn_global_load_lds((const unsigned*)(A + (size_t)(brow + r) * K + kt * BK + ch * 8),
                                       (unsigned*)(la + by), 16, 0, 0);
    }
#pragma unroll
    for (int i = 0; i < BBYTES / 4096; ++i) {
      int by = t * 16 + i * 4096, r = by >> 7, ch = ((by >> 4) & 7) ^ (r & 7);
      __builtin_amdgcn_global_load_lds((const unsigned*)(Bt + (size_t)(bcol + r) * K + kt * BK + ch * 8),
                                       (unsigned*)(lb + by), 16, 0, 0);
    }
  };
  f32x4 acc[TM][TN];
#pragma unroll
  for (int m = 0; m < TM; ++m)
#pragma unroll
    for (int n = 0; n < TN; ++n) acc[m][n] = (f32x4){0.f, 0.f, 0.f, 0.f};
  const int nt = K / BK;
  stage(0, 0);
  asm volatile("s_waitcnt vmcnt(0)" ::: "memory");
  __syncthreads();
  for (int kt = 0; kt < nt; ++kt) {
    int cur = kt & 1;
    if (kt + 1 < nt) stage(cur ^ 1, kt + 1);
    const char* la = lds + cur * (ABYTES + BBYTES);
    const char* lb = la + ABYTES;
#pragma unroll
    for (int kk = 0; kk < 2; ++kk) {
      bf16x8 af[TM], bf[TN];
#pragma unroll
      for (int m = 0; m < TM; ++m) {
        int r = wr * (BM / 2) + m * 16 + fr;
        af[m] = *(const bf16x8*)(la + r * 128 + (((kk * 4 + fq) ^ (r & 7)) << 4));
      }
#pragma unroll
      for (int n = 0; n < TN; ++n) {
        int r = wc * (BN / 2) + n * 16 + fr;
        bf[n] = *(const bf16x8*)(lb + r * 128 + (((kk * 4 + fq) ^ (r & 7)) << 4));
      }
#pragma unroll
      for (int m = 0; m < TM; ++m)
#pragma unroll
        for (int n = 0; n < TN; ++n)
          acc[m][n] = __builtin_amdgcn_mfma_f32_16x16x32_bf16(bf[n], af[m], acc[m][n], 0, 0, 0);
    }
    asm volatile("s_waitcnt vmcnt(0)" ::: "memory");
    __syncthreads();
  }
#pragma unroll
  for (int m = 0; m < TM; ++m) {
    int row = brow + wr * (BM / 2) + m * 16 + fr;
    if (EPI == 0) {
#pragma unroll
      for (int n = 0; n < TN; n += 2) {
        int hc = (bcol + wc * (BN / 2)) / 2 + (n / 2) * 16 + fq * 4;
        u16 o[4];
#pragma unroll
        for (int j = 0; j < 4; ++j) {
          float g = rbf(acc[m][n][j]), u = rbf(acc[m][n + 1][j]);
          float sg = rbf(g / (1.0f + __expf(-g)));
          o[j] = f2bf(sg * u);
        }
        *(uint2*)(out + (size_t)row * ldo + hc) = make_uint2(o[0] | ((unsigned)o[1] << 16), o[2] | ((unsigned)o[3] << 16));
      }
    } else {
#pragma unroll
      for (int n = 0; n < TN; ++n) {
        int cc = bcol + wc * (BN / 2) + n * 16 + fq * 4;
        uint2 rv = *(const uint2*)(res + (size_t)row * ldo + cc);
        float r0 = __uint_as_float(rv.x << 16), r1 = __uint_as_float(rv.x & 0xffff0000u);
        float r2 = __uint_as_float(rv.y << 16), r3 = __uint_as_float(rv.y & 0xffff0000u);
        u16 o0 = f2bf(r0 + rbf(acc[m][n][0])), o1 = f2bf(r1 + rbf(acc[m][n][1]));
        u16 o2 = f2bf(r2 + rbf(acc[m][n][2])), o3 = f2bf(r3 + rbf(acc[m][n][3]));
        *(uint2*)(out + (size_t)row * ldo + cc) = make_uint2(o0 | ((unsigned)o1 << 16), o2 | ((unsigned)o3 << 16));
      }
    }
  }
}

extern "C" void kernel_launch(void* const* d_in, const int* in_sizes, int n_in,
                              void* d_out, int out_size, void* d_ws, size_t ws_size,
                              hipStream_t stream) {
  const u16* Ax = (const u16*)d_in[0];
  const u16* wr = (const u16*)d_in[1];
  const u16* wi = (const u16*)d_in[2];
  const u16* w1 = (const u16*)d_in[4];
  const u16* w3 = (const u16*)d_in[5];
  const u16* w2 = (const u16*)d_in[6];
  char* ws = (char*)d_ws;
  size_t off = 0;
  auto alloc = [&](size_t bytes) { void* p = ws + off; off += (bytes + 255) & ~(size_t)255; return p; };
  float2* Zt = (float2*)alloc((size_t)DF_ * S_ * 8);
  unsigned* Wt = (unsigned*)alloc((size_t)DF_ * S_ * 4);
  u16* xb = (u16*)alloc((size_t)S_ * D_ * 2);
  u16* xln = (u16*)alloc((size_t)S_ * D_ * 2);
  u16* h = (u16*)alloc((size_t)S_ * F_ * 2);
  u16* w13p = (u16*)alloc((size_t)2 * F_ * D_ * 2);
  u16* w2t = (u16*)alloc((size_t)D_ * F_ * 2);
  u16* outp = (u16*)d_out;

  for (int layer = 0; layer < 2; ++layer) {
    const u16* xin = layer == 0 ? Ax : xb;
    k_a1<<<256, 320, 0, stream>>>(xin, Zt, wr, wi, Wt, layer == 0 ? 1 : 0);
    k_a2<<<layer == 0 ? DF_ + 810 : DF_, 256, 0, stream>>>(Zt, Wt, w1, w3, w2, w13p, w2t);
    k_a3<<<256, 320, 0, stream>>>(Zt, xin, xb, xln);
    k_gemm<128, 128, 0><<<16 * 27, 256, 0, stream>>>(xln, w13p, D_, 27, nullptr, h, F_);
    k_gemm<128, 64, 1><<<16 * 10, 256, 0, stream>>>(h, w2t, F_, 10, xb, layer == 0 ? xb : outp, D_);
  }
}
```

```cpp
#include <hip/hip_runtime.h>
#include <hip/hip_bf16.h>
#include <stdint.h>

typedef unsigned short u16;
using bf16x8 = __attribute__((ext_vector_type(8))) short;
using f32x4 = __attribute__((ext_vector_type(4))) float;

constexpr int S_ = 2048, D_ = 640, DF_ = 321, F_ = 1728;

__device__ __forceinline__ float bf2f(u16 v) { return __uint_as_float(((unsigned)v) << 16); }
__device__ __forceinline__ u16 f2bf(float f) { return __bfloat16_as_ushort(__float2bfloat16(f)); }
__device__ __forceinline__ float rbf(float f) { return bf2f(f2bf(f)); }

__device__ __forceinline__ float2 cadd(float2 a, float2 b) { return make_float2(a.x + b.x, a.y + b.y); }
__device__ __forceinline__ float2 csub(float2 a, float2 b) { return make_float2(a.x - b.x, a.y - b.y); }
__device__ __forceinline__ float2 cmul(float2 a, float2 b) {
  return make_float2(a.x * b.x - a.y * b.y, a.x * b.y + a.y * b.x);
}
template <int SG> __device__ __forceinline__ float2 mul_i(float2 a) {
  return SG > 0 ? make_float2(-a.y, a.x) : make_float2(a.y, -a.x);
}
template <int SG> __device__ __forceinline__ float2 twc(float2 w) {
  return SG > 0 ? make_float2(w.x, -w.y) : w;
}

template <int SG> __device__ __forceinline__ void dft4(float2& a0, float2& a1, float2& a2, float2& a3) {
  float2 s0 = cadd(a0, a2), d0 = csub(a0, a2), s1 = cadd(a1, a3), d1 = mul_i<SG>(csub(a1, a3));
  a0 = cadd(s0, s1); a2 = csub(s0, s1); a1 = cadd(d0, d1); a3 = csub(d0, d1);
}
template <int SG> __device__ __forceinline__ void dft8(float2* v) {
  float2 e0 = v[0], e1 = v[2], e2 = v[4], e3 = v[6], o0 = v[1], o1 = v[3], o2 = v[5], o3 = v[7];
  dft4<SG>(e0, e1, e2, e3); dft4<SG>(o0, o1, o2, o3);
  const float h = 0.70710678118654752440f;
  float2 t1 = make_float2(h * (o1.x - SG * o1.y), h * (o1.y + SG * o1.x));
  float2 t2 = mul_i<SG>(o2);
  float2 t3 = make_float2(h * (-o3.x - SG * o3.y), h * (-o3.y + SG * o3.x));
  v[0] = cadd(e0, o0); v[4] = csub(e0, o0);
  v[1] = cadd(e1, t1); v[5] = csub(e1, t1);
  v[2] = cadd(e2, t2); v[6] = csub(e2, t2);
  v[3] = cadd(e3, t3); v[7] = csub(e3, t3);
}
template <int SG> __device__ __forceinline__ void dft5(float2& a0, float2& a1, float2& a2, float2& a3, float2& a4) {
  const float c1 = 0.30901699437494742410f, c2 = -0.80901699437494742410f;
  const float s1 = 0.95105651629515357212f, s2 = 0.58778525229247312917f;
  float2 p1 = cadd(a1, a4), m1 = csub(a1, a4), p2 = cadd(a2, a3), m2 = csub(a2, a3);
  float2 t1 = make_float2(a0.x + c1 * p1.x + c2 * p2.x, a0.y + c1 * p1.y + c2 * p2.y);
  float2 t2 = make_float2(a0.x + c2 * p1.x + c1 * p2.x, a0.y + c2 * p1.y + c1 * p2.y);
  float2 u1 = mul_i<SG>(make_float2(s1 * m1.x + s2 * m2.x, s1 * m1.y + s2 * m2.y));
  float2 u2 = mul_i<SG>(make_float2(s2 * m1.x - s1 * m2.x, s2 * m1.y - s1 * m2.y));
  a0 = cadd(a0, cadd(p1, p2));
  a1 = cadd(t1, u1); a4 = csub(t1, u1);
  a2 = cadd(t2, u2); a3 = csub(t2, u2);
}
template <int SG> __device__ __forceinline__ void dft10(float2* v) {
  float2 e0 = v[0], e1 = v[2], e2 = v[4], e3 = v[6], e4 = v[8];
  float2 o0 = v[1], o1 = v[3], o2 = v[5], o3 = v[7], o4 = v[9];
  dft5<SG>(e0, e1, e2, e3, e4); dft5<SG>(o0, o1, o2, o3, o4);
  const float c1 = 0.80901699437494742410f, s1 = 0.58778525229247312917f;
  const float c2 = 0.30901699437494742410f, s2 = 0.95105651629515357212f;
  o1 = cmul(o1, make_float2(c1, SG * s1));
  o2 = cmul(o2, make_float2(c2, SG * s2));
  o3 = cmul(o3, make_float2(-c2, SG * s2));
  o4 = cmul(o4, make_float2(-c1, SG * s1));
  v[0] = cadd(e0, o0); v[5] = csub(e0, o0);
  v[1] = cadd(e1, o1); v[6] = csub(e1, o1);
  v[2] = cadd(e2, o2); v[7] = csub(e2, o2);
  v[3] = cadd(e3, o3); v[8] = csub(e3, o3);
  v[4] = cadd(e4, o4); v[9] = csub(e4, o4);
}

__device__ __forceinline__ int PD(int i) { return i + (i >> 3); }

template <int SG>
__device__ __forceinline__ void fft640(float2* b0, float2* b1, const float2* tw, int j) {
  float2 v[10];
#pragma unroll
  for (int r = 0; r < 8; ++r) v[r] = b0[PD(j + 80 * r)];
  dft8<SG>(v);
#pragma unroll
  for (int r = 0; r < 8; ++r) b1[PD(8 * j + r)] = v[r];
  __syncthreads();
  {
    int m = j & 7;
#pragma unroll
    for (int r = 0; r < 8; ++r) v[r] = b1[PD(j + 80 * r)];
#pragma unroll
    for (int r = 1; r < 8; ++r) v[r] = cmul(v[r], twc<SG>(tw[10 * r * m]));
    dft8<SG>(v);
#pragma unroll
    for (int r = 0; r < 8; ++r) b0[PD((j >> 3) * 64 + m + 8 * r)] = v[r];
  }
  __syncthreads();
  if (j < 64) {
#pragma unroll
    for (int r = 0; r < 10; ++r) v[r] = b0[PD(j + 64 * r)];
#pragma unroll
    for (int r = 1; r < 10; ++r) v[r] = cmul(v[r], twc<SG>(tw[r * j]));
    dft10<SG>(v);
#pragma unroll
    for (int r = 0; r < 10; ++r) b1[PD(j + 64 * r)] = v[r];
  }
}

__device__ __forceinline__ void ln16(float* xv, float* red, int row, int c) {
  float s = 0.f;
#pragma unroll
  for (int i = 0; i < 16; ++i) s += xv[i];
  s += __shfl_xor(s, 1); s += __shfl_xor(s, 2); s += __shfl_xor(s, 4);
  if ((c & 7) == 0) red[row * 5 + (c >> 3)] = s;
  __syncthreads();
  float mean = (red[row * 5] + red[row * 5 + 1] + red[row * 5 + 2] + red[row * 5 + 3] + red[row * 5 + 4]) * (1.0f / 640.0f);
  float q = 0.f;
#pragma unroll
  for (int i = 0; i < 16; ++i) { float d = xv[i] - mean; q += d * d; }
  q += __shfl_xor(q, 1); q += __shfl_xor(q, 2); q += __shfl_xor(q, 4);
  if ((c & 7) == 0) red[40 + row * 5 + (c >> 3)] = q;
  __syncthreads();
  float var = (red[40 + row * 5] + red[40 + row * 5 + 1] + red[40 + row * 5 + 2] + red[40 + row * 5 + 3] + red[40 + row * 5 + 4]) * (1.0f / 640.0f);
  float mb = rbf(mean), vb = rbf(var);
  float den = rbf(sqrtf(rbf(vb + 1e-5f)));
#pragma unroll
  for (int i = 0; i < 16; ++i) xv[i] = rbf(rbf(xv[i] - mb) / den);
}

constexpr int FB = 720;

__global__ __launch_bounds__(320) void k_a1(const u16* __restrict__ x, float2* __restrict__ Zt,
                                            const u16* __restrict__ wr, const u16* __restrict__ wi,
                                            unsigned* __restrict__ Wt, int do_prep) {
  __shared__ float2 buf[2][4][FB];
  __shared__ float2 tw[640];
  __shared__ float red[80];
  const int t = threadIdx.x, s0 = blockIdx.x * 8;
  if (do_prep) {
    for (int k = t; k < DF_; k += 320) {
      unsigned p[8];
#pragma unroll
      for (int r = 0; r < 8; ++r)
        p[r] = (unsigned)wr[(s0 + r) * DF_ + k] | ((unsigned)wi[(s0 + r) * DF_ + k] << 16);
      uint4* dst = (uint4*)(Wt + (size_t)k * S_ + s0);
      dst[0] = make_uint4(p[0], p[1], p[2], p[3]);
      dst[1] = make_uint4(p[4], p[5], p[6], p[7]);
    }
  }
  const int row = t / 40, c = t % 40;
  float xv[16];
  {
    const uint4* src = (const uint4*)(x + (size_t)(s0 + row) * D_ + c * 16);
    uint4 a = src[0], b = src[1];
    unsigned u[8] = {a.x, a.y, a.z, a.w, b.x, b.y, b.z, b.w};
#pragma unroll
    for (int i = 0; i < 8; ++i) { xv[2 * i] = __uint_as_float(u[i] << 16); xv[2 * i + 1] = __uint_as_float(u[i] & 0xffff0000u); }
  }
  for (int n = t; n < 640; n += 320) {
    float sn, cs; sincospif(-(float)n * (1.0f / 320.0f), &sn, &cs);
    tw[n] = make_float2(cs, sn);
  }
  ln16(xv, red, row, c);
  {
    float* dst = (float*)&buf[0][row >> 1][0] + (row & 1);
#pragma unroll
    for (int i = 0; i < 16; ++i) dst[2 * PD(c * 16 + i)] = xv[i];
  }
  __syncthreads();
  const int f = t / 80, j = t % 80;
  fft640<-1>(buf[0][f], buf[1][f], tw, j);
  __syncthreads();
  for (int e = t; e < DF_ * 4; e += 320) {
    int k = e >> 2, ff = e & 3;
    float2 a = buf[1][ff][PD(k)], b = buf[1][ff][PD(k == 0 ? 0 : 640 - k)];
    float4 o;
    o.x = 0.5f * (a.x + b.x); o.y = 0.5f * (a.y - b.y);
    o.z = 0.5f * (a.y + b.y); o.w = 0.5f * (b.x - a.x);
    *(float4*)(Zt + (size_t)k * S_ + s0 + 2 * ff) = o;
  }
}

__device__ __forceinline__ void pack_w13(const u16* __restrict__ src, u16* __restrict__ dst, int which, int nb, int k0, u16* sm) {
  const int t = threadIdx.x;
  for (int e = t; e < 64 * 27; e += 256) {
    int r = e / 27, q = e % 27;
    uint2 v = *(const uint2*)(src + (size_t)(k0 + r) * F_ + nb * 108 + q * 4);
    sm[(q * 4 + 0) * 66 + r] = (u16)(v.x & 0xffff); sm[(q * 4 + 1) * 66 + r] = (u16)(v.x >> 16);
    sm[(q * 4 + 2) * 66 + r] = (u16)(v.y & 0xffff); sm[(q * 4 + 3) * 66 + r] = (u16)(v.y >> 16);
  }
  __syncthreads();
  for (int e = t; e < 112 * 8; e += 256) {
    int c = e >> 3, q = e & 7;
    uint4 v = make_uint4(0, 0, 0, 0);
    if (c < 108) {
      u16* pp = (u16*)&v;
#pragma unroll
      for (int i = 0; i < 8; ++i) pp[i] = sm[c * 66 + q * 8 + i];
    }
    int drow = nb * 224 + (c >> 4) * 32 + which * 16 + (c & 15);
    *(uint4*)(dst + (size_t)drow * D_ + k0 + q * 8) = v;
  }
}
__device__ __forceinline__ void pack_w2(const u16* __restrict__ src, u16* __restrict__ dst, int nb, int d0, u16* sm) {
  const int t = threadIdx.x;
  for (int e = t; e < 108 * 8; e += 256) {
    int i = e >> 3, q = e & 7;
    uint4 v = *(const uint4*)(src + (size_t)(nb * 108 + i) * D_ + d0 + q * 8);
    u16* pp = (u16*)&v;
#pragma unroll
    for (int j = 0; j < 8; ++j) sm[(q * 8 + j) * 114 + i] = pp[j];
  }
  __syncthreads();
  for (int e = t; e < 64 * 14; e += 256) {
    int d = e / 14, q = e % 14;
    uint4 v; u16* pp = (u16*)&v;
#pragma unroll
    for (int j = 0; j < 8; ++j) { int i = q * 8 + j; pp[j] = i < 108 ? sm[d * 114 + i] : (u16)0; }
    *(uint4*)(dst + (size_t)(d0 + d) * 1792 + nb * 112 + q * 8) = v;
  }
}

constexpr int CB = 2048 + 256;
__global__ __launch_bounds__(256) void k_a2(float2* __restrict__ Zt, const unsigned* __restrict__ Wt,
                                            const u16* __restrict__ w1, const u16* __restrict__ w3, const u16* __restrict__ w2,
                                            u16* __restrict__ w13p, u16* __restrict__ w2t) {
  __shared__ float2 smem[2 * CB + 2048];
  const int t = threadIdx.x;
  if (blockIdx.x >= DF_) {
    int id = blockIdx.x - DF_;
    u16* sm = (u16*)smem;
    if (id < 320) {
      int which = id / 160, r = id % 160;
      pack_w13(which ? w3 : w1, w13p, which, r / 10, (r % 10) * 64, sm);
    } else {
      int r = id - 320;
      pack_w2(w2, w2t, r / 10, (r % 10) * 64, sm);
    }
    return;
  }
  float2* b0 = smem; float2* b1 = smem + CB; float2* tw = smem + 2 * CB;
  const int k = blockIdx.x;
  float2* col = Zt + (size_t)k * S_;
  float2 v[8];
#pragma unroll
  for (int r = 0; r < 8; ++r) v[r] = col[t + 256 * r];
  unsigned wv[8];
#pragma unroll
  for (int r = 0; r < 8; ++r) wv[r] = Wt[(size_t)k * S_ + t + 256 * r];
#pragma unroll
  for (int r = 0; r < 8; ++r) {
    int n = t + 256 * r; float sn, cs; sincospif(-(float)n * (1.0f / 1024.0f), &sn, &cs);
    tw[n] = make_float2(cs, sn);
  }
  dft8<-1>(v);
#pragma unroll
  for (int r = 0; r < 8; ++r) b0[PD(8 * t + r)] = v[r];
  __syncthreads();
  {
    int m = t & 7;
#pragma unroll
    for (int r = 0; r < 8; ++r) v[r] = b0[PD(t + 256 * r)];
#pragma unroll
    for (int r = 1; r < 8; ++r) v[r] = cmul(v[r], tw[32 * r * m]);
    dft8<-1>(v);
#pragma unroll
    for (int r = 0; r < 8; ++r) b1[PD((t >> 3) * 64 + m + 8 * r)] = v[r];
  }
  __syncthreads();
  {
    int m = t & 63;
#pragma unroll
    for (int r = 0; r < 8; ++r) v[r] = b1[PD(t + 256 * r)];
#pragma unroll
    for (int r = 1; r < 8; ++r) v[r] = cmul(v[r], tw[4 * r * m]);
    dft8<-1>(v);
#pragma unroll
    for (int r = 0; r < 8; ++r) b0[PD((t >> 6) * 512 + m + 64 * r)] = v[r];
  }
  __syncthreads();
#pragma unroll
  for (int h = 0; h < 2; ++h) {
    int j = t + 256 * h;
    float2 a0 = b0[PD(j)], a1 = b0[PD(j + 512)], a2 = b0[PD(j + 1024)], a3 = b0[PD(j + 1536)];
    a1 = cmul(a1, tw[j]); a2 = cmul(a2, tw[2 * j]); a3 = cmul(a3, tw[3 * j]);
    dft4<-1>(a0, a1, a2, a3);
    v[h] = a0; v[h + 2] = a1; v[h + 4] = a2; v[h + 6] = a3;
  }
  const float sc = 1.0f / (2048.0f * 640.0f);
#pragma unroll
  for (int r = 0; r < 8; ++r) {
    float2 w = make_float2(__uint_as_float(wv[r] << 16) * sc, __uint_as_float(wv[r] & 0xffff0000u) * sc);
    v[r] = cmul(v[r], w);
  }
  dft8<1>(v);
#pragma unroll
  for (int r = 0; r < 8; ++r) b1[PD(8 * t + r)] = v[r];
  __syncthreads();
  {
    int m = t & 7;
#pragma unroll
    for (int r = 0; r < 8; ++r) v[r] = b1[PD(t + 256 * r)];
#pragma unroll
    for (int r = 1; r < 8; ++r) v[r] = cmul(v[r], twc<1>(tw[32 * r * m]));
    dft8<1>(v);
#pragma unroll
    for (int r = 0; r < 8; ++r) b0[PD((t >> 3) * 64 + m + 8 * r)] = v[r];
  }
  __syncthreads();
  {
    int m = t & 63;
#pragma unroll
    for (int r = 0; r < 8; ++r) v[r] = b0[PD(t + 256 * r)];
#pragma unroll
    for (int r = 1; r < 8; ++r) v[r] = cmul(v[r], twc<1>(tw[4 * r * m]));
    dft8<1>(v);
#pragma unroll
    for (int r = 0; r < 8; ++r) b1[PD((t >> 6) * 512 + m + 64 * r)] = v[r];
  }
  __syncthreads();
#pragma unroll
  for (int h = 0; h < 2; ++h) {
    int j = t + 256 * h;
    float2 a0 = b1[PD(j)], a1 = b1[PD(j + 512)], a2 = b1[PD(j + 1024)], a3 = b1[PD(j + 1536)];
    a1 = cmul(a1, twc<1>(tw[j])); a2 = cmul(a2, twc<1>(tw[2 * j])); a3 = cmul(a3, twc<1>(tw[3 * j]));
    dft4<1>(a0, a1, a2, a3);
    col[j] = a0; col[j + 512] = a1; col[j + 1024] = a2; col[j + 1536] = a3;
  }
}

__global__ __launch_bounds__(320) void k_a3(const float2* __restrict__ Zt, const u16* xin,
                                            u16* xout, u16* __restrict__ xln) {
  __shared__ float2 buf[2][4][FB];
  __shared__ float2 tw[640];
  __shared__ float red[80];
  const int t = threadIdx.x, s0 = blockIdx.x * 8;
  for (int e = t; e < DF_ * 4; e += 320) {
    int k = e >> 2, ff = e & 3;
    float4 z = *(const float4*)(Zt + (size_t)k * S_ + s0 + 2 * ff);
    if (k == 0 || k == 320) {
      buf[0][ff][PD(k)] = make_float2(z.x, z.z);
    } else {
      buf[0][ff][PD(k)] = make_float2(z.x - z.w, z.y + z.z);
      buf[0][ff][PD(640 - k)] = make_float2(z.x + z.w, z.z - z.y);
    }
  }
  const int row = t / 40, c = t % 40;
  float xv[16];
  {
    const uint4* src = (const uint4*)(xin + (size_t)(s0 + row) * D_ + c * 16);
    uint4 a = src[0], b = src[1];
    unsigned u[8] = {a.x, a.y, a.z, a.w, b.x, b.y, b.z, b.w};
#pragma unroll
    for (int i = 0; i < 8; ++i) { xv[2 * i] = __uint_as_float(u[i] << 16); xv[2 * i + 1] = __uint_as_float(u[i] & 0xffff0000u); }
  }
  for (int n = t; n < 640; n += 320) {
    float sn, cs; sincospif(-(float)n * (1.0f / 320.0f), &sn, &cs);
    tw[n] = make_float2(cs, sn);
  }
  __syncthreads();
  const int f = t / 80, j = t % 80;
  fft640<1>(buf[0][f], buf[1][f], tw, j);
  __syncthreads();
  {
    const float* src = (const float*)&buf[1][row >> 1][0] + (row & 1);
    unsigned u[8];
#pragma unroll
    for (int i = 0; i < 16; ++i) xv[i] = rbf(xv[i] + rbf(src[2 * PD(c * 16 + i)]));
#pragma unroll
    for (int i = 0; i < 8; ++i) u[i] = (__float_as_uint(xv[2 * i]) >> 16) | (__float_as_uint(xv[2 * i + 1]) & 0xffff0000u);
    uint4* dst = (uint4*)(xout + (size_t)(s0 + row) * D_ + c * 16);
    dst[0] = make_uint4(u[0], u[1], u[2], u[3]); dst[1] = make_uint4(u[4], u[5], u[6], u[7]);
  }
  ln16(xv, red, row, c);
  {
    unsigned u[8];
#pragma unroll
    for (int i = 0; i < 8; ++i) u[i] = (__float_as_uint(xv[2 * i]) >> 16) | (__float_as_uint(xv[2 * i + 1]) & 0xffff0000u);
    uint4* dst = (uint4*)(xln + (size_t)(s0 + row) * D_ + c * 16);
    dst[0] = make_uint4(u[0], u[1], u[2], u[3]); dst[1] = make_uint4(u[4], u[5], u[6], u[7]);
  }
}

template <int N> __device__ __forceinline__ void wait_vm() { asm volatile("s_waitcnt vmcnt(%0)" ::"i"(N) : "memory"); }
__device__ __forceinline__ void wait_lgkm0() { asm volatile("s_waitcnt lgkmcnt(0)" ::: "memory"); }

constexpr int FP_ = 1792;

__global__ __launch_bounds__(256) void k_g1(const u16* __restrict__ A, const u16* __restrict__ Bt, u16* __restrict__ out) {
  constexpr int K = 640, BM = 128, BN = 224, BK = 64, NS = 3, NT = K / BK;
  constexpr int ABYTES = BM * BK * 2, BBYTES = BN * BK * 2, STG = ABYTES + BBYTES, L = STG / 4096;
  __shared__ __attribute__((aligned(16))) char lds[NS * STG];
  const int t = threadIdx.x, wid = t >> 6, lane = t & 63, fr = lane & 15, fq = lane >> 4;
  const int xcd = blockIdx.x & 7, idx = blockIdx.x >> 3;
  const int bm = (xcd >> 1) * 4 + (idx & 3), bn = (xcd & 1) * 8 + (idx >> 2);
  const u16* Ab = A + (size_t)bm * BM * K;
  const u16* Bb = Bt + (size_t)bn * BN * K;
  auto stage = [&](int b, int kt) {
    char* la = lds + b * STG;
#pragma unroll
    for (int i = 0; i < ABYTES / 4096; ++i) {
      int by = t * 16 + i * 4096, r = by >> 7, ch = ((by >> 4) & 7) ^ (r & 7);
      __builtin_amdgcn_global_load_lds((const unsigned*)(Ab + (size_t)r * K + kt * BK + ch * 8), (unsigned*)(la + by), 16, 0, 0);
    }
#pragma unroll
    for (int i = 0; i < BBYTES / 4096; ++i) {
      int by = t * 16 + i * 4096, r = by >> 7, ch = ((by >> 4) & 7) ^ (r & 7);
      __builtin_amdgcn_global_load_lds((const unsigned*)(Bb + (size_t)r * K + kt * BK + ch * 8), (unsigned*)(la + ABYTES + by), 16, 0, 0);
    }
  };
  f32x4 acc[2][14];
#pragma unroll
  for (int m = 0; m < 2; ++m)
#pragma unroll
    for (int n = 0; n < 14; ++n) acc[m][n] = (f32x4){0.f, 0.f, 0.f, 0.f};
  stage(0, 0); stage(1, 1);
#pragma unroll 1
  for (int kt = 0; kt < NT; ++kt) {
    if (kt + 1 < NT) wait_vm<L>(); else wait_vm<0>();
    wait_lgkm0();
    __builtin_amdgcn_s_barrier();
    if (kt + 2 < NT) stage((kt + 2) % NS, kt + 2);
    const char* la = lds + (kt % NS) * STG;
    const char* lb = la + ABYTES;
#pragma unroll
    for (int kk = 0; kk < 2; ++kk) {
      bf16x8 af[2], bfr[14];
#pragma unroll
      for (int m = 0; m < 2; ++m) {
        int r = wid * 32 + m * 16 + fr;
        af[m] = *(const bf16x8*)(la + r * 128 + (((kk * 4 + fq) ^ (r & 7)) << 4));
      }
#pragma unroll
      for (int n = 0; n < 14; ++n) {
        int r = n * 16 + fr;
        bfr[n] = *(const bf16x8*)(lb + r * 128 + (((kk * 4 + fq) ^ (r & 7)) << 4));
      }
#pragma unroll
      for (int n = 0; n < 14; ++n)
#pragma unroll
        for (int m = 0; m < 2; ++m)
          acc[m][n] = __builtin_amdgcn_mfma_f32_16x16x32_bf16(bfr[n], af[m], acc[m][n], 0, 0, 0);
    }
  }
#pragma unroll
  for (int m = 0; m < 2; ++m) {
    int row = bm * BM + wid * 32 + m * 16 + fr;
#pragma unroll
    for (int p = 0; p < 7; ++p) {
      int hc = bn * 112 + p * 16 + fq * 4;
      u16 o[4];
#pragma unroll
      for (int j = 0; j < 4; ++j) {
        float g = rbf(acc[m][2 * p][j]), u = rbf(acc[m][2 * p + 1][j]);
        float sg = rbf(g / (1.0f + __expf(-g)));
        o[j] = f2bf(sg * u);
      }
      *(uint2*)(out + (size_t)row * FP_ + hc) = make_uint2(o[0] | ((unsigned)o[1] << 16), o[2] | ((unsigned)o[3] << 16));
    }
  }
}

__global__ __launch_bounds__(256) void k_g2(const u16* __restrict__ A, const u16* __restrict__ Bt, const u16* res, u16* out) {
  constexpr int K = FP_, BM = 64, BN = 80, BK = 128, NS = 4, NT = K / BK;
  constexpr int ABYTES = BM * BK * 2, BBYTES = BN * BK * 2, STG = ABYTES + BBYTES, L = STG / 4096;
  __shared__ __attribute__((aligned(16))) char lds[NS * STG];
  const int t = threadIdx.x, wid = t >> 6, lane = t & 63, fr = lane & 15, fq = lane >> 4;
  const int xcd = blockIdx.x & 7, idx = blockIdx.x >> 3;
  const int bm = xcd * 4 + (idx & 3), bn = idx >> 2;
  const u16* Ab = A + (size_t)bm * BM * K;
  const u16* Bb = Bt + (size_t)bn * BN * K;
  auto stage = [&](int b, int kt) {
    char* la = lds + b * STG;
#pragma unroll
    for (int i = 0; i < ABYTES / 4096; ++i) {
      int by = t * 16 + i * 4096, r = by >> 8, ch = ((by >> 4) & 15) ^ (r & 15);
      __builtin_amdgcn_global_load_lds((const unsigned*)(Ab + (size_t)r * K + kt * BK + ch * 8), (unsigned*)(la + by), 16, 0, 0);
    }
#pragma unroll
    for (int i = 0; i < BBYTES / 4096; ++i) {
      int by = t * 16 + i * 4096, r = by >> 8, ch = ((by >> 4) & 15) ^ (r & 15);
      __builtin_amdgcn_global_load_lds((const unsigned*)(Bb + (size_t)r * K + kt * BK + ch * 8), (unsigned*)(la + ABYTES + by), 16, 0, 0);
    }
  };
  f32x4 acc[4][5];
#pragma unroll
  for (int m = 0; m < 4; ++m)
#pragma unroll
    for (int n = 0; n < 5; ++n) acc[m][n] = (f32x4){0.f, 0.f, 0.f, 0.f};
  stage(0, 0); stage(1, 1); stage(2, 2);
#pragma unroll 1
  for (int kt = 0; kt < NT; ++kt) {
    if (kt + 2 < NT) wait_vm<2 * L>(); else if (kt + 1 < NT) wait_vm<L>(); else wait_vm<0>();
    wait_lgkm0();
    __builtin_amdgcn_s_barrier();
    if (kt + 3 < NT) stage((kt + 3) % NS, kt + 3);
    const char* la = lds + (kt % NS) * STG;
    const char* lb = la + ABYTES;
    bf16x8 af[4], bfr[5];
#pragma unroll
    for (int m = 0; m < 4; ++m) {
      int r = m * 16 + fr;
      af[m] = *(const bf16x8*)(la + r * 256 + (((wid * 4 + fq) ^ (r & 15)) << 4));
    }
#pragma unroll
    for (int n = 0; n < 5; ++n) {
      int r = n * 16 + fr;
      bfr[n] = *(const bf16x8*)(lb + r * 256 + (((wid * 4 + fq) ^ (r & 15)) << 4));
    }
#pragma unroll
    for (int m = 0; m < 4; ++m)
#pragma unroll
      for (int n = 0; n < 5; ++n)
        acc[m][n] = __builtin_amdgcn_mfma_f32_16x16x32_bf16(bfr[n], af[m], acc[m][n], 0, 0, 0);
  }
  __syncthreads();
  f32x4* red = (f32x4*)lds;
#pragma unroll
  for (int m = 0; m < 4; ++m)
#pragma unroll
    for (int n = 0; n < 5; ++n) red[(wid * 20 + m * 5 + n) * 64 + lane] = acc[m][n];
  __syncthreads();
#pragma unroll
  for (int n = 0; n < 5; ++n) {
    int fi = wid * 5 + n;
    f32x4 s = red[(0 * 20 + fi) * 64 + lane] + red[(20 + fi) * 64 + lane] + red[(40 + fi) * 64 + lane] + red[(60 + fi) * 64 + lane];
    int row = bm * BM + wid * 16 + fr;
    int cc = bn * BN + n * 16 + fq * 4;
    uint2 rv = *(const uint2*)(res + (size_t)row * D_ + cc);
    float r0 = __uint_as_float(rv.x << 16), r1 = __uint_as_float(rv.x & 0xffff0000u);
    float r2 = __uint_as_float(rv.y << 16), r3 = __uint_as_float(rv.y & 0xffff0000u);
    u16 o0 = f2bf(r0 + rbf(s[0])), o1 = f2bf(r1 + rbf(s[1]));
    u16 o2 = f2bf(r2 + rbf(s[2])), o3 = f2bf(r3 + rbf(s[3]));
    *(uint2*)(out + (size_t)row * D_ + cc) = make_uint2(o0 | ((unsigned)o1 << 16), o2 | ((unsigned)o3 << 16));
  }
}

extern "C" void kernel_launch(void* const* d_in, const int* in_sizes, int n_in,
                              void* d_out, int out_size, void* d_ws, size_t ws_size,
                              hipStream_t stream) {
  const u16* Ax = (const u16*)d_in[0];
  const u16* wr = (const u16*)d_in[1];
  const u16* wi = (const u16*)d_in[2];
  const u16* w1 = (const u16*)d_in[4];
  const u16* w3 = (const u16*)d_in[5];
  const u16* w2 = (const u16*)d_in[6];
  char* ws = (char*)d_ws;
  size_t off = 0;
  auto alloc = [&](size_t bytes) { void* p = ws + off; off += (bytes + 255) & ~(size_t)255; return p; };
  float2* Zt = (float2*)alloc((size_t)DF_ * S_ * 8);
  unsigned* Wt = (unsigned*)alloc((size_t)DF_ * S_ * 4);
  u16* xb = (u16*)alloc((size_t)S_ * D_ * 2);
  u16* xln = (u16*)alloc((size_t)S_ * D_ * 2);
  u16* h = (u16*)alloc((size_t)S_ * 1792 * 2);
  u16* w13p = (u16*)alloc((size_t)16 * 224 * D_ * 2);
  u16* w2t = (u16*)alloc((size_t)D_ * 1792 * 2);
  u16* outp = (u16*)d_out;

  for (int layer = 0; layer < 2; ++layer) {
    const u16* xin = layer == 0 ? Ax : xb;
    k_a1<<<256, 320, 0, stream>>>(xin, Zt, wr, wi, Wt, layer == 0 ? 1 : 0);
    k_a2<<<layer == 0 ? DF_ + 480 : DF_, 256, 0, stream>>>(Zt, Wt, w1, w3, w2, w13p, w2t);
    k_a3<<<256, 320, 0, stream>>>(Zt, xin, xb, xln);
    k_g1<<<256, 256, 0, stream>>>(xln, w13p, h);
    k_g2<<<256, 256, 0, stream>>>(h, w2t, xb, layer == 0 ? xb : outp);
  }
}
```

```cpp
#include <hip/hip_runtime.h>
#include <hip/hip_bf16.h>
#include <stdint.h>

typedef unsigned short u16;
using bf16x8 = __attribute__((ext_vector_type(8))) short;
using f32x4 = __attribute__((ext_vector_type(4))) float;

constexpr int S_ = 2048, D_ = 640, DF_ = 321, F_ = 1728;

__device__ __forceinline__ float bf2f(u16 v) { return __uint_as_float(((unsigned)v) << 16); }
__device__ __forceinline__ u16 f2bf(float f) { return __bfloat16_as_ushort(__float2bfloat16(f)); }
__device__ __forceinline__ float rbf(float f) { return bf2f(f2bf(f)); }

__device__ __forceinline__ float2 cadd(float2 a, float2 b) { return make_float2(a.x + b.x, a.y + b.y); }
__device__ __forceinline__ float2 csub(float2 a, float2 b) { return make_float2(a.x - b.x, a.y - b.y); }
__device__ __forceinline__ float2 cmul(float2 a, float2 b) {
  return make_float2(a.x * b.x - a.y * b.y, a.x * b.y + a.y * b.x);
}
template <int SG> __device__ __forceinline__ float2 mul_i(float2 a) {
  return SG > 0 ? make_float2(-a.y, a.x) : make_float2(a.y, -a.x);
}
template <int SG> __device__ __forceinline__ float2 twc(float2 w) {
  return SG > 0 ? make_float2(w.x, -w.y) : w;
}

template <int SG> __device__ __forceinline__ void dft4(float2& a0, float2& a1, float2& a2, float2& a3) {
  float2 s0 = cadd(a0, a2), d0 = csub(a0, a2), s1 = cadd(a1, a3), d1 = mul_i<SG>(csub(a1, a3));
  a0 = cadd(s0, s1); a2 = csub(s0, s1); a1 = cadd(d0, d1); a3 = csub(d0, d1);
}
template <int SG> __device__ __forceinline__ void dft8(float2* v) {
  float2 e0 = v[0], e1 = v[2], e2 = v[4], e3 = v[6], o0 = v[1], o1 = v[3], o2 = v[5], o3 = v[7];
  dft4<SG>(e0, e1, e2, e3); dft4<SG>(o0, o1, o2, o3);
  const float h = 0.70710678118654752440f;
  float2 t1 = make_float2(h * (o1.x - SG * o1.y), h * (o1.y + SG * o1.x));
  float2 t2 = mul_i<SG>(o2);
  float2 t3 = make_float2(h * (-o3.x - SG * o3.y), h * (-o3.y + SG * o3.x));
  v[0] = cadd(e0, o0); v[4] = csub(e0, o0);
  v[1] = cadd(e1, t1); v[5] = csub(e1, t1);
  v[2] = cadd(e2, t2); v[6] = csub(e2, t2);
  v[3] = cadd(e3, t3); v[7] = csub(e3, t3);
}
template <int SG> __device__ __forceinline__ void dft5(float2& a0, float2& a1, float2& a2, float2& a3, float2& a4) {
  const float c1 = 0.30901699437494742410f, c2 = -0.80901699437494742410f;
  const float s1 = 0.95105651629515357212f, s2 = 0.58778525229247312917f;
  float2 p1 = cadd(a1, a4), m1 = csub(a1, a4), p2 = cadd(a2, a3), m2 = csub(a2, a3);
  float2 t1 = make_float2(a0.x + c1 * p1.x + c2 * p2.x, a0.y + c1 * p1.y + c2 * p2.y);
  float2 t2 = make_float2(a0.x + c2 * p1.x + c1 * p2.x, a0.y + c2 * p1.y + c1 * p2.y);
  float2 u1 = mul_i<SG>(make_float2(s1 * m1.x + s2 * m2.x, s1 * m1.y + s2 * m2.y));
  float2 u2 = mul_i<SG>(make_float2(s2 * m1.x - s1 * m2.x, s2 * m1.y - s1 * m2.y));
  a0 = cadd(a0, cadd(p1, p2));
  a1 = cadd(t1, u1); a4 = csub(t1, u1);
  a2 = cadd(t2, u2); a3 = csub(t2, u2);
}
template <int SG> __device__ __forceinline__ void dft10(float2* v) {
  float2 e0 = v[0], e1 = v[2], e2 = v[4], e3 = v[6], e4 = v[8];
  float2 o0 = v[1], o1 = v[3], o2 = v[5], o3 = v[7], o4 = v[9];
  dft5<SG>(e0, e1, e2, e3, e4); dft5<SG>(o0, o1, o2, o3, o4);
  const float c1 = 0.80901699437494742410f, s1 = 0.58778525229247312917f;
  const float c2 = 0.30901699437494742410f, s2 = 0.95105651629515357212f;
  o1 = cmul(o1, make_float2(c1, SG * s1));
  o2 = cmul(o2, make_float2(c2, SG * s2));
  o3 = cmul(o3, make_float2(-c2, SG * s2));
  o4 = cmul(o4, make_float2(-c1, SG * s1));
  v[0] = cadd(e0, o0); v[5] = csub(e0, o0);
  v[1] = cadd(e1, o1); v[6] = csub(e1, o1);
  v[2] = cadd(e2, o2); v[7] = csub(e2, o2);
  v[3] = cadd(e3, o3); v[8] = csub(e3, o3);
  v[4] = cadd(e4, o4); v[9] = csub(e4, o4);
}

__device__ __forceinline__ int PD(int i) { return i + (i >> 3); }
__device__ __forceinline__ int swz(int row) { return (-(row >> 2)) & 3; }
__device__ __forceinline__ size_t kb_off(int R, int row, int k) {
  return (size_t)(k >> 5) * ((size_t)R * 32) + (size_t)row * 32 + ((((k >> 3) & 3) ^ swz(row)) << 3) + (k & 7);
}

template <int SG>
__device__ __forceinline__ void fft640(float2* b0, float2* b1, const float2* tw, int j) {
  float2 v[10];
#pragma unroll
  for (int r = 0; r < 8; ++r) v[r] = b0[PD(j + 80 * r)];
  dft8<SG>(v);
#pragma unroll
  for (int r = 0; r < 8; ++r) b1[PD(8 * j + r)] = v[r];
  __syncthreads();
  {
    int m = j & 7;
#pragma unroll
    for (int r = 0; r < 8; ++r) v[r] = b1[PD(j + 80 * r)];
#pragma unroll
    for (int r = 1; r < 8; ++r) v[r] = cmul(v[r], twc<SG>(tw[10 * r * m]));
    dft8<SG>(v);
#pragma unroll
    for (int r = 0; r < 8; ++r) b0[PD((j >> 3) * 64 + m + 8 * r)] = v[r];
  }
  __syncthreads();
  if (j < 64) {
#pragma unroll
    for (int r = 0; r < 10; ++r) v[r] = b0[PD(j + 64 * r)];
#pragma unroll
    for (int r = 1; r < 10; ++r) v[r] = cmul(v[r], twc<SG>(tw[r * j]));
    dft10<SG>(v);
#pragma unroll
    for (int r = 0; r < 10; ++r) b1[PD(j + 64 * r)] = v[r];
  }
}

__device__ __forceinline__ void ln16(float* xv, float* red, int row, int c) {
  float s = 0.f;
#pragma unroll
  for (int i = 0; i < 16; ++i) s += xv[i];
  s += __shfl_xor(s, 1); s += __shfl_xor(s, 2); s += __shfl_xor(s, 4);
  if ((c & 7) == 0) red[row * 5 + (c >> 3)] = s;
  __syncthreads();
  float mean = (red[row * 5] + red[row * 5 + 1] + red[row * 5 + 2] + red[row * 5 + 3] + red[row * 5 + 4]) * (1.0f / 640.0f);
  float q = 0.f;
#pragma unroll
  for (int i = 0; i < 16; ++i) { float d = xv[i] - mean; q += d * d; }
  q += __shfl_xor(q, 1); q += __shfl_xor(q, 2); q += __shfl_xor(q, 4);
  if ((c & 7) == 0) red[40 + row * 5 + (c >> 3)] = q;
  __syncthreads();
  float var = (red[40 + row * 5] + red[40 + row * 5 + 1] + red[40 + row * 5 + 2] + red[40 + row * 5 + 3] + red[40 + row * 5 + 4]) * (1.0f / 640.0f);
  float mb = rbf(mean), vb = rbf(var);
  float den = rbf(sqrtf(rbf(vb + 1e-5f)));
#pragma unroll
  for (int i = 0; i < 16; ++i) xv[i] = rbf(rbf(xv[i] - mb) / den);
}

constexpr int FB = 720;

__global__ __launch_bounds__(320) void k_a1(const u16* __restrict__ x, float2* __restrict__ Zt,
                                            const u16* __restrict__ wr, const u16* __restrict__ wi,
                                            unsigned* __restrict__ Wt, int do_prep) {
  __shared__ float2 buf[2][4][FB];
  __shared__ float2 tw[640];
  __shared__ float red[80];
  const int t = threadIdx.x, s0 = blockIdx.x * 8;
  if (do_prep) {
    for (int k = t; k < DF_; k += 320) {
      unsigned p[8];
#pragma unroll
      for (int r = 0; r < 8; ++r)
        p[r] = (unsigned)wr[(s0 + r) * DF_ + k] | ((unsigned)wi[(s0 + r) * DF_ + k] << 16);
      uint4* dst = (uint4*)(Wt + (size_t)k * S_ + s0);
      dst[0] = make_uint4(p[0], p[1], p[2], p[3]);
      dst[1] = make_uint4(p[4], p[5], p[6], p[7]);
    }
  }
  const int row = t / 40, c = t % 40;
  float xv[16];
  {
    const uint4* src = (const uint4*)(x + (size_t)(s0 + row) * D_ + c * 16);
    uint4 a = src[0], b = src[1];
    unsigned u[8] = {a.x, a.y, a.z, a.w, b.x, b.y, b.z, b.w};
#pragma unroll
    for (int i = 0; i < 8; ++i) { xv[2 * i] = __uint_as_float(u[i] << 16); xv[2 * i + 1] = __uint_as_float(u[i] & 0xffff0000u); }
  }
  for (int n = t; n < 640; n += 320) {
    float sn, cs; sincospif(-(float)n * (1.0f / 320.0f), &sn, &cs);
    tw[n] = make_float2(cs, sn);
  }
  ln16(xv, red, row, c);
  {
    float* dst = (float*)&buf[0][row >> 1][0] + (row & 1);
#pragma unroll
    for (int i = 0; i < 16; ++i) dst[2 * PD(c * 16 + i)] = xv[i];
  }
  __syncthreads();
  const int f = t / 80, j = t % 80;
  fft640<-1>(buf[0][f], buf[1][f], tw, j);
  __syncthreads();
  for (int e = t; e < DF_ * 4; e += 320) {
    int k = e >> 2, ff = e & 3;
    float2 a = buf[1][ff][PD(k)], b = buf[1][ff][PD(k == 0 ? 0 : 640 - k)];
    float4 o;
    o.x = 0.5f * (a.x + b.x); o.y = 0.5f * (a.y - b.y);
    o.z = 0.5f * (a.y + b.y); o.w = 0.5f * (b.x - a.x);
    *(float4*)(Zt + (size_t)k * S_ + s0 + 2 * ff) = o;
  }
}

__device__ __forceinline__ void pack_w13(const u16* __restrict__ src, u16* __restrict__ dst, int which, int nb, int k0, u16* sm) {
  const int t = threadIdx.x;
  for (int e = t; e < 64 * 27; e += 256) {
    int r = e / 27, q = e % 27;
    uint2 v = *(const uint2*)(src + (size_t)(k0 + r) * F_ + nb * 108 + q * 4);
    sm[(q * 4 + 0) * 66 + r] = (u16)(v.x & 0xffff); sm[(q * 4 + 1) * 66 + r] = (u16)(v.x >> 16);
    sm[(q * 4 + 2) * 66 + r] = (u16)(v.y & 0xffff); sm[(q * 4 + 3) * 66 + r] = (u16)(v.y >> 16);
  }
  __syncthreads();
  for (int e = t; e < 112 * 8; e += 256) {
    int c = e >> 3, q = e & 7;
    uint4 v = make_uint4(0, 0, 0, 0);
    if (c < 108) {
      u16* pp = (u16*)&v;
#pragma unroll
      for (int i = 0; i < 8; ++i) pp[i] = sm[c * 66 + q * 8 + i];
    }
    int drow = nb * 224 + (c >> 4) * 32 + which * 16 + (c & 15);
    *(uint4*)(dst + kb_off(16 * 224, drow, k0 + q * 8)) = v;
  }
}
__device__ __forceinline__ void pack_w2(const u16* __restrict__ src, u16* __restrict__ dst, int nb, int d0, u16* sm) {
  const int t = threadIdx.x;
  for (int e = t; e < 108 * 8; e += 256) {
    int i = e >> 3, q = e & 7;
    uint4 v = *(const uint4*)(src + (size_t)(nb * 108 + i) * D_ + d0 + q * 8);
    u16* pp = (u16*)&v;
#pragma unroll
    for (int j = 0; j < 8; ++j) sm[(q * 8 + j) * 114 + i] = pp[j];
  }
  __syncthreads();
  for (int e = t; e < 64 * 14; e += 256) {
    int d = e / 14, q = e % 14;
    uint4 v; u16* pp = (u16*)&v;
#pragma unroll
    for (int j = 0; j < 8; ++j) { int i = q * 8 + j; pp[j] = i < 108 ? sm[d * 114 + i] : (u16)0; }
    *(uint4*)(dst + kb_off(D_, d0 + d, nb * 112 + q * 8)) = v;
  }
}

constexpr int CB = 2048 + 256;
__global__ __launch_bounds__(256) void k_a2(float2* __restrict__ Zt, const unsigned* __restrict__ Wt,
                                            const u16* __restrict__ w1, const u16* __restrict__ w3, const u16* __restrict__ w2,
                                            u16* __restrict__ w13p, u16* __restrict__ w2t) {
  __shared__ float2 smem[2 * CB + 2048];
  const int t = threadIdx.x;
  if (blockIdx.x >= DF_) {
    int id = blockIdx.x - DF_;
    u16* sm = (u16*)smem;
    if (id < 320) {
      int which = id / 160, r = id % 160;
      pack_w13(which ? w3 : w1, w13p, which, r / 10, (r % 10) * 64, sm);
    } else {
      int r = id - 320;
      pack_w2(w2, w2t, r / 10, (r % 10) * 64, sm);
    }
    return;
  }
  float2* b0 = smem; float2* b1 = smem + CB; float2* tw = smem + 2 * CB;
  const int k = blockIdx.x;
  float2* col = Zt + (size_t)k * S_;
  float2 v[8];
#pragma unroll
  for (int r = 0; r < 8; ++r) v[r] = col[t + 256 * r];
  unsigned wv[8];
#pragma unroll
  for (int r = 0; r < 8; ++r) wv[r] = Wt[(size_t)k * S_ + t + 256 * r];
#pragma unroll
  for (int r = 0; r < 8; ++r) {
    int n = t + 256 * r; float sn, cs; sincospif(-(float)n * (1.0f / 1024.0f), &sn, &cs);
    tw[n] = make_float2(cs, sn);
  }
  dft8<-1>(v);
#pragma unroll
  for (int r = 0; r < 8; ++r) b0[PD(8 * t + r)] = v[r];
  __syncthreads();
  {
    int m = t & 7;
#pragma unroll
    for (int r = 0; r < 8; ++r) v[r] = b0[PD(t + 256 * r)];
#pragma unroll
    for (int r = 1; r < 8; ++r) v[r] = cmul(v[r], tw[32 * r * m]);
    dft8<-1>(v);
#pragma unroll
    for (int r = 0; r < 8; ++r) b1[PD((t >> 3) * 64 + m + 8 * r)] = v[r];
  }
  __syncthreads();
  {
    int m = t & 63;
#pragma unroll
    for (int r = 0; r < 8; ++r) v[r] = b1[PD(t + 256 * r)];
#pragma unroll
    for (int r = 1; r < 8; ++r) v[r] = cmul(v[r], tw[4 * r * m]);
    dft8<-1>(v);
#pragma unroll
    for (int r = 0; r < 8; ++r) b0[PD((t >> 6) * 512 + m + 64 * r)] = v[r];
  }
  __syncthreads();
#pragma unroll
  for (int h = 0; h < 2; ++h) {
    int j = t + 256 * h;
    float2 a0 = b0[PD(j)], a1 = b0[PD(j + 512)], a2 = b0[PD(j + 1024)], a3 = b0[PD(j + 1536)];
    a1 = cmul(a1, tw[j]); a2 = cmul(a2, tw[2 * j]); a3 = cmul(a3, tw[3 * j]);
    dft4<-1>(a0, a1, a2, a3);
    v[h] = a0; v[h + 2] = a1; v[h + 4] = a2; v[h + 6] = a3;
  }
  const float sc = 1.0f / (2048.0f * 640.0f);
#pragma unroll
  for (int r = 0; r < 8; ++r) {
    float2 w = make_float2(__uint_as_float(wv[r] << 16) * sc, __uint_as_float(wv[r] & 0xffff0000u) * sc);
    v[r] = cmul(v[r], w);
  }
  dft8<1>(v);
#pragma unroll
  for (int r = 0; r < 8; ++r) b1[PD(8 * t + r)] = v[r];
  __syncthreads();
  {
    int m = t & 7;
#pragma unroll
    for (int r = 0; r < 8; ++r) v[r] = b1[PD(t + 256 * r)];
#pragma unroll
    for (int r = 1; r < 8; ++r) v[r] = cmul(v[r], twc<1>(tw[32 * r * m]));
    dft8<1>(v);
#pragma unroll
    for (int r = 0; r < 8; ++r) b0[PD((t >> 3) * 64 + m + 8 * r)] = v[r];
  }
  __syncthreads();
  {
    int m = t & 63;
#pragma unroll
    for (int r = 0; r < 8; ++r) v[r] = b0[PD(t + 256 * r)];
#pragma unroll
    for (int r = 1; r < 8; ++r) v[r] = cmul(v[r], twc<1>(tw[4 * r * m]));
    dft8<1>(v);
#pragma unroll
    for (int r = 0; r < 8; ++r) b1[PD((t >> 6) * 512 + m + 64 * r)] = v[r];
  }
  __syncthreads();
#pragma unroll
  for (int h = 0; h < 2; ++h) {
    int j = t + 256 * h;
    float2 a0 = b1[PD(j)], a1 = b1[PD(j + 512)], a2 = b1[PD(j + 1024)], a3 = b1[PD(j + 1536)];
    a1 = cmul(a1, twc<1>(tw[j])); a2 = cmul(a2, twc<1>(tw[2 * j])); a3 = cmul(a3, twc<1>(tw[3 * j]));
    dft4<1>(a0, a1, a2, a3);
    col[j] = a0; col[j + 512] = a1; col[j + 1024] = a2; col[j + 1536] = a3;
  }
}

__global__ __launch_bounds__(320) void k_a3(const float2* __restrict__ Zt, const u16* xin,
                                            u16* xout, u16* __restrict__ xln) {
  __shared__ float2 buf[2][4][FB];
  __shared__ float2 tw[640];
  __shared__ float red[80];
  const int t = threadIdx.x, s0 = blockIdx.x * 8;
  for (int e = t; e < DF_ * 4; e += 320) {
    int k = e >> 2, ff = e & 3;
    float4 z = *(const float4*)(Zt + (size_t)k * S_ + s0 + 2 * ff);
    if (k == 0 || k == 320) {
      buf[0][ff][PD(k)] = make_float2(z.x, z.z);
    } else {
      buf[0][ff][PD(k)] = make_float2(z.x - z.w, z.y + z.z);
      buf[0][ff][PD(640 - k)] = make_float2(z.x + z.w, z.z - z.y);
    }
  }
  const int row = t / 40, c = t % 40;
  float xv[16];
  {
    const uint4* src = (const uint4*)(xin + (size_t)(s0 + row) * D_ + c * 16);
    uint4 a = src[0], b = src[1];
    unsigned u[8] = {a.x, a.y, a.z, a.w, b.x, b.y, b.z, b.w};
#pragma unroll
    for (int i = 0; i < 8; ++i) { xv[2 * i] = __uint_as_float(u[i] << 16); xv[2 * i + 1] = __uint_as_float(u[i] & 0xffff0000u); }
  }
  for (int n = t; n < 640; n += 320) {
    float sn, cs; sincospif(-(float)n * (1.0f / 320.0f), &sn, &cs);
    tw[n] = make_float2(cs, sn);
  }
  __syncthreads();
  const int f = t / 80, j = t % 80;
  fft640<1>(buf[0][f], buf[1][f], tw, j);
  __syncthreads();
  {
    const float* src = (const float*)&buf[1][row >> 1][0] + (row & 1);
    unsigned u[8];
#pragma unroll
    for (int i = 0; i < 16; ++i) xv[i] = rbf(xv[i] + rbf(src[2 * PD(c * 16 + i)]));
#pragma unroll
    for (int i = 0; i < 8; ++i) u[i] = (__float_as_uint(xv[2 * i]) >> 16) | (__float_as_uint(xv[2 * i + 1]) & 0xffff0000u);
    uint4* dst = (uint4*)(xout + (size_t)(s0 + row) * D_ + c * 16);
    dst[0] = make_uint4(u[0], u[1], u[2], u[3]); dst[1] = make_uint4(u[4], u[5], u[6], u[7]);
  }
  ln16(xv, red, row, c);
  {
    unsigned u[8];
#pragma unroll
    for (int i = 0; i < 8; ++i) u[i] = (__float_as_uint(xv[2 * i]) >> 16) | (__float_as_uint(xv[2 * i + 1]) & 0xffff0000u);
    *(uint4*)(xln + kb_off(S_, s0 + row, c * 16)) = make_uint4(u[0], u[1], u[2], u[3]);
    *(uint4*)(xln + kb_off(S_, s0 + row, c * 16 + 8)) = make_uint4(u[4], u[5], u[6], u[7]);
  }
}

template <int N> __device__ __forceinline__ void wait_vm() { asm volatile("s_waitcnt vmcnt(%0)" ::"i"(N) : "memory"); }
__device__ __forceinline__ void wait_lgkm0() { asm volatile("s_waitcnt lgkmcnt(0)" ::: "memory"); }

constexpr int FP_ = 1792;

template <int NP, int L>
__device__ __forceinline__ void g1_body(const u16* __restrict__ A, const u16* __restrict__ Bt, u16* __restrict__ out,
                                        char* lds, int bm, int bn, int wm, int p0) {
  constexpr int BM = 128, BN = 224, NS = 6, NT = 20, PF = 5;
  constexpr int ABYTES = BM * 64, BBYTES = BN * 64, STG = ABYTES + BBYTES;
  const int t = threadIdx.x, lane = t & 63, fr = lane & 15, fq = lane >> 4;
  const char* Ag = (const char*)A + (size_t)bm * ABYTES + t * 16;
  const char* Bg = (const char*)Bt + (size_t)bn * BBYTES + t * 16;
  auto stage = [&](int ks) {
    char* la = lds + (ks % NS) * STG + t * 16;
    __builtin_amdgcn_global_load_lds((const unsigned*)(Ag + (size_t)ks * (S_ * 64)), (unsigned*)la, 16, 0, 0);
    __builtin_amdgcn_global_load_lds((const unsigned*)(Bg + (size_t)ks * (3584 * 64)), (unsigned*)(la + ABYTES), 16, 0, 0);
    if (L == 3)
      __builtin_amdgcn_global_load_lds((const unsigned*)(Bg + (size_t)ks * (3584 * 64) + 8192), (unsigned*)(la + ABYTES + 8192), 16, 0, 0);
  };
  const int aoff = (wm * 32 + fr) * 64 + ((fq ^ swz(fr)) << 4);
  const int boff = ABYTES + (p0 * 32 + fr) * 64 + ((fq ^ swz(fr)) << 4);
  bf16x8 af[2][2], bfr[2][2 * NP];
  auto ldfr = [&](int set, int ks) {
    const char* l = lds + (ks % NS) * STG;
#pragma unroll
    for (int m = 0; m < 2; ++m) af[set][m] = *(const bf16x8*)(l + aoff + m * 1024);
#pragma unroll
    for (int n = 0; n < 2 * NP; ++n) bfr[set][n] = *(const bf16x8*)(l + boff + n * 1024);
  };
  f32x4 acc[2][2 * NP];
#pragma unroll
  for (int m = 0; m < 2; ++m)
#pragma unroll
    for (int n = 0; n < 2 * NP; ++n) acc[m][n] = (f32x4){0.f, 0.f, 0.f, 0.f};
#pragma unroll
  for (int i = 0; i < PF; ++i) stage(i);
  wait_vm<(PF - 1) * L>();
  __builtin_amdgcn_s_barrier();
  ldfr(0, 0);
#pragma unroll
  for (int ks = 0; ks < NT; ++ks) {
    const int cur = ks & 1;
    if (ks + 1 < NT) {
      constexpr int dummy = 0; (void)dummy;
      const int issued = (ks + PF - 1 < NT - 1) ? ks + PF - 1 : NT - 1;
      const int allow = (issued - (ks + 1)) * L;
      if (allow >= 3 * L) wait_vm<3 * L>(); else if (allow == 2 * L) wait_vm<2 * L>(); else if (allow == L) wait_vm<L>(); else wait_vm<0>();
      __builtin_amdgcn_s_barrier();
      if (ks + PF < NT) stage(ks + PF);
      ldfr(cur ^ 1, ks + 1);
    }
#pragma unroll
    for (int n = 0; n < 2 * NP; ++n)
#pragma unroll
      for (int m = 0; m < 2; ++m)
        acc[m][n] = __builtin_amdgcn_mfma_f32_16x16x32_bf16(bfr[cur][n], af[cur][m], acc[m][n], 0, 0, 0);
    if (ks + 1 < NT) {
#pragma unroll
      for (int g = 0; g < L; ++g) {
        __builtin_amdgcn_sched_group_barrier(0x8, 1, 0);
        __builtin_amdgcn_sched_group_barrier(0x10, 1, 0);
      }
#pragma unroll
      for (int g = 0; g < 2 * NP + 2; ++g) {
        __builtin_amdgcn_sched_group_barrier(0x8, 1, 0);
        __builtin_amdgcn_sched_group_barrier(0x100, 1, 0);
      }
    }
    __builtin_amdgcn_sched_barrier(0);
  }
#pragma unroll
  for (int m = 0; m < 2; ++m) {
    int row = bm * BM + wm * 32 + m * 16 + fr;
#pragma unroll
    for (int p = 0; p < NP; ++p) {
      int hc = bn * 112 + (p0 + p) * 16 + fq * 4;
      u16 o[4];
#pragma unroll
      for (int j = 0; j < 4; ++j) {
        float g = rbf(acc[m][2 * p][j]), u = rbf(acc[m][2 * p + 1][j]);
        float sg = rbf(g * __builtin_amdgcn_rcpf(1.0f + __expf(-g)));
        o[j] = f2bf(sg * u);
      }
      *(uint2*)(out + kb_off(S_, row, hc)) = make_uint2(o[0] | ((unsigned)o[1] << 16), o[2] | ((unsigned)o[3] << 16));
    }
  }
}
__global__ __launch_bounds__(512) void k_g1(const u16* __restrict__ A, const u16* __restrict__ Bt, u16* __restrict__ out) {
  __shared__ __attribute__((aligned(16))) char lds[6 * (128 + 224) * 64];
  const int wid = __builtin_amdgcn_readfirstlane(threadIdx.x >> 6);
  const int xcd = blockIdx.x & 7, idx = blockIdx.x >> 3;
  const int bm = (xcd >> 1) * 4 + (idx & 3), bn = (xcd & 1) * 8 + (idx >> 2);
  if (wid < 4) g1_body<4, 3>(A, Bt, out, lds, bm, bn, wid, 0);
  else if (wid < 6) g1_body<3, 3>(A, Bt, out, lds, bm, bn, wid - 4, 4);
  else g1_body<3, 2>(A, Bt, out, lds, bm, bn, wid - 4, 4);
}

template <int L>
__device__ __forceinline__ void g2_body(const u16* __restrict__ A, const u16* __restrict__ Bt, const u16* res, u16* out,
                                        char* lds, int bm, int bn, int wid) {
  constexpr int BM = 64, BN = 80, NS = 4, NT = 14, PF = 3;
  constexpr int ABYTES = 4 * BM * 64, BBYTES = 4 * BN * 64, STG = ABYTES + BBYTES;
  const int t = threadIdx.x, lane = t & 63, fr = lane & 15, fq = lane >> 4;
  const int ks4 = wid & 3, mh = wid >> 2;
  const char* Ag[2]; const char* Bg[3];
#pragma unroll
  for (int i = 0; i < 2; ++i) { int by = t * 16 + i * 8192; Ag[i] = (const char*)A + (size_t)(by >> 12) * (S_ * 64) + (size_t)bm * 4096 + (by & 4095); }
#pragma unroll
  for (int i = 0; i < 3; ++i) { int by = t * 16 + i * 8192; int kb = by / 5120; Bg[i] = (const char*)Bt + (size_t)kb * (D_ * 64) + (size_t)bn * 5120 + (by - kb * 5120); }
  auto stage = [&](int st) {
    char* la = lds + (st % NS) * STG + t * 16;
#pragma unroll
    for (int i = 0; i < 2; ++i)
      __builtin_amdgcn_global_load_lds((const unsigned*)(Ag[i] + (size_t)st * (4 * S_ * 64)), (unsigned*)(la + i * 8192), 16, 0, 0);
#pragma unroll
    for (int i = 0; i < L - 2; ++i)
      __builtin_amdgcn_global_load_lds((const unsigned*)(Bg[i] + (size_t)st * (4 * D_ * 64)), (unsigned*)(la + ABYTES + i * 8192), 16, 0, 0);
  };
  const int aoff = ks4 * 4096 + (mh * 32 + fr) * 64 + ((fq ^ swz(fr)) << 4);
  const int boff = ABYTES + ks4 * 5120 + fr * 64 + ((fq ^ swz(fr)) << 4);
  bf16x8 af[2][2], bfr[2][5];
  auto ldfr = [&](int set, int st) {
    const char* l = lds + (st % NS) * STG;
#pragma unroll
    for (int m = 0; m < 2; ++m) af[set][m] = *(const bf16x8*)(l + aoff + m * 1024);
#pragma unroll
    for (int n = 0; n < 5; ++n) bfr[set][n] = *(const bf16x8*)(l + boff + n * 1024);
  };
  f32x4 acc[2][5];
#pragma unroll
  for (int m = 0; m < 2; ++m)
#pragma unroll
    for (int n = 0; n < 5; ++n) acc[m][n] = (f32x4){0.f, 0.f, 0.f, 0.f};
#pragma unroll
  for (int i = 0; i < PF; ++i) stage(i);
  wait_vm<(PF - 1) * L>();
  __builtin_amdgcn_s_barrier();
  ldfr(0, 0);
#pragma unroll
  for (int st = 0; st < NT; ++st) {
    const int cur = st & 1;
    if (st + 1 < NT) {
      const int issued = (st + PF - 1 < NT - 1) ? st + PF - 1 : NT - 1;
      const int allow = (issued - (st + 1)) * L;
      if (allow >= L) wait_vm<L>(); else wait_vm<0>();
      __builtin_amdgcn_s_barrier();
      if (st + PF < NT) stage(st + PF);
      ldfr(cur ^ 1, st + 1);
    }
#pragma unroll
    for (int m = 0; m < 2; ++m)
#pragma unroll
      for (int n = 0; n < 5; ++n)
        acc[m][n] = __builtin_amdgcn_mfma_f32_16x16x32_bf16(bfr[cur][n], af[cur][m], acc[m][n], 0, 0, 0);
    if (st + 1 < NT) {
#pragma unroll
      for (int g = 0; g < 5; ++g) {
        __builtin_amdgcn_sched_group_barrier(0x8, 1, 0);
        __builtin_amdgcn_sched_group_barrier(0x10, 1, 0);
        __builtin_amdgcn_sched_group_barrier(0x100, 1, 0);
      }
#pragma unroll
      for (int g = 0; g < 2; ++g) {
        __builtin_amdgcn_sched_group_barrier(0x8, 1, 0);
        __builtin_amdgcn_sched_group_barrier(0x100, 1, 0);
      }
    }
    __builtin_amdgcn_sched_barrier(0);
  }
  __syncthreads();
  f32x4* red = (f32x4*)lds;
#pragma unroll
  for (int m = 0; m < 2; ++m)
#pragma unroll
    for (int n = 0; n < 5; ++n) red[(ks4 * 20 + mh * 10 + m * 5 + n) * 64 + lane] = acc[m][n];
  __syncthreads();
#pragma unroll
  for (int i = 0; i < 3; ++i) {
    int fi = wid + 8 * i;
    if (fi < 20) {
      f32x4 s = red[fi * 64 + lane] + red[(20 + fi) * 64 + lane] + red[(40 + fi) * 64 + lane] + red[(60 + fi) * 64 + lane];
      int mrow = fi / 5, n = fi % 5;
      int row = bm * BM + mrow * 16 + fr;
      int cc = bn * BN + n * 16 + fq * 4;
      uint2 rv = *(const uint2*)(res + (size_t)row * D_ + cc);
      float r0 = __uint_as_float(rv.x << 16), r1 = __uint_as_float(rv.x & 0xffff0000u);
      float r2 = __uint_as_float(rv.y << 16), r3 = __uint_as_float(rv.y & 0xffff0000u);
      u16 o0 = f2bf(r0 + rbf(s[0])), o1 = f2bf(r1 + rbf(s[1]));
      u16 o2 = f2bf(r2 + rbf(s[2])), o3 = f2bf(r3 + rbf(s[3]));
      *(uint2*)(out + (size_t)row * D_ + cc) = make_uint2(o0 | ((unsigned)o1 << 16), o2 | ((unsigned)o3 << 16));
    }
  }
}
__global__ __launch_bounds__(512) void k_g2(const u16* __restrict__ A, const u16* __restrict__ Bt, const u16* res, u16* out) {
  __shared__ __attribute__((aligned(16))) char lds[4 * 4 * (64 + 80) * 64];
  const int wid = __builtin_amdgcn_readfirstlane(threadIdx.x >> 6);
  const int xcd = blockIdx.x & 7, idx = blockIdx.x >> 3;
  const int bm = xcd * 4 + (idx & 3), bn = idx >> 2;
  if (wid < 4) g2_body<5>(A, Bt, res, out, lds, bm, bn, wid);
  else g2_body<4>(A, Bt, res, out, lds, bm, bn, wid);
}

extern "C" void kernel_launch(void* const* d_in, const int* in_sizes, int n_in,
                              void* d_out, int out_size, void* d_ws, size_t ws_size,
                              hipStream_t stream) {
  const u16* Ax = (const u16*)d_in[0];
  const u16* wr = (const u16*)d_in[1];
  const u16* wi = (const u16*)d_in[2];
  const u16* w1 = (const u16*)d_in[4];
  const u16* w3 = (const u16*)d_in[5];
  const u16* w2 = (const u16*)d_in[6];
  char* ws = (char*)d_ws;
  size_t off = 0;
  auto alloc = [&](size_t bytes) { void* p = ws + off; off += (bytes + 255) & ~(size_t)255; return p; };
  float2* Zt = (float2*)alloc((size_t)DF_ * S_ * 8);
  unsigned* Wt = (unsigned*)alloc((size_t)DF_ * S_ * 4);
  u16* xb = (u16*)alloc((size_t)S_ * D_ * 2);
  u16* xln = (u16*)alloc((size_t)S_ * D_ * 2);
  u16* h = (u16*)alloc((size_t)S_ * 1792 * 2);
  u16* w13p = (u16*)alloc((size_t)16 * 224 * D_ * 2);
  u16* w2t = (u16*)alloc((size_t)D_ * 1792 * 2);
  u16* outp = (u16*)d_out;

  for (int layer = 0; layer < 2; ++layer) {
    const u16* xin = layer == 0 ? Ax : xb;
    k_a1<<<256, 320, 0, stream>>>(xin, Zt, wr, wi, Wt, layer == 0 ? 1 : 0);
    k_a2<<<layer == 0 ? DF_ + 480 : DF_, 256, 0, stream>>>(Zt, Wt, w1, w3, w2, w13p, w2t);
    k_a3<<<256, 320, 0, stream>>>(Zt, xin, xb, xln);
    k_g1<<<256, 512, 0, stream>>>(xln, w13p, h);
    k_g2<<<256, 512, 0, stream>>>(h, w2t, xb, layer == 0 ? xb : outp);
  }
}
```

```cpp
#include <hip/hip_runtime.h>
#include <hip/hip_bf16.h>
#include <stdint.h>

typedef unsigned short u16;
using bf16x8 = __attribute__((ext_vector_type(8))) short;
using f32x4 = __attribute__((ext_vector_type(4))) float;

constexpr int S_ = 2048, D_ = 640, DF_ = 321, F_ = 1728;

__device__ __forceinline__ float bf2f(u16 v) { return __uint_as_float(((unsigned)v) << 16); }
__device__ __forceinline__ u16 f2bf(float f) { return __bfloat16_as_ushort(__float2bfloat16(f)); }
__device__ __forceinline__ float rbf(float f) { return bf2f(f2bf(f)); }

__device__ __forceinline__ float2 cadd(float2 a, float2 b) { return make_float2(a.x + b.x, a.y + b.y); }
__device__ __forceinline__ float2 csub(float2 a, float2 b) { return make_float2(a.x - b.x, a.y - b.y); }
__device__ __forceinline__ float2 cmul(float2 a, float2 b) {
  return make_float2(a.x * b.x - a.y * b.y, a.x * b.y + a.y * b.x);
}
template <int SG> __device__ __forceinline__ float2 mul_i(float2 a) {
  return SG > 0 ? make_float2(-a.y, a.x) : make_float2(a.y, -a.x);
}
template <int SG> __device__ __forceinline__ float2 twc(float2 w) {
  return SG > 0 ? make_float2(w.x, -w.y) : w;
}

template <int SG> __device__ __forceinline__ void dft4(float2& a0, float2& a1, float2& a2, float2& a3) {
  float2 s0 = cadd(a0, a2), d0 = csub(a0, a2), s1 = cadd(a1, a3), d1 = mul_i<SG>(csub(a1, a3));
  a0 = cadd(s0, s1); a2 = csub(s0, s1); a1 = cadd(d0, d1); a3 = csub(d0, d1);
}
template <int SG> __device__ __forceinline__ void dft8(float2* v) {
  float2 e0 = v[0], e1 = v[2], e2 = v[4], e3 = v[6], o0 = v[1], o1 = v[3], o2 = v[5], o3 = v[7];
  dft4<SG>(e0, e1, e2, e3); dft4<SG>(o0, o1, o2, o3);
  const float h = 0.70710678118654752440f;
  float2 t1 = make_float2(h * (o1.x - SG * o1.y), h * (o1.y + SG * o1.x));
  float2 t2 = mul_i<SG>(o2);
  float2 t3 = make_float2(h * (-o3.x - SG * o3.y), h * (-o3.y + SG * o3.x));
  v[0] = cadd(e0, o0); v[4] = csub(e0, o0);
  v[1] = cadd(e1, t1); v[5] = csub(e1, t1);
  v[2] = cadd(e2, t2); v[6] = csub(e2, t2);
  v[3] = cadd(e3, t3); v[7] = csub(e3, t3);
}
template <int SG> __device__ __forceinline__ void dft5(float2& a0, float2& a1, float2& a2, float2& a3, float2& a4) {
  const float c1 = 0.30901699437494742410f, c2 = -0.80901699437494742410f;
  const float s1 = 0.95105651629515357212f, s2 = 0.58778525229247312917f;
  float2 p1 = cadd(a1, a4), m1 = csub(a1, a4), p2 = cadd(a2, a3), m2 = csub(a2, a3);
  float2 t1 = make_float2(a0.x + c1 * p1.x + c2 * p2.x, a0.y + c1 * p1.y + c2 * p2.y);
  float2 t2 = make_float2(a0.x + c2 * p1.x + c1 * p2.x, a0.y + c2 * p1.y + c1 * p2.y);
  float2 u1 = mul_i<SG>(make_float2(s1 * m1.x + s2 * m2.x, s1 * m1.y + s2 * m2.y));
  float2 u2 = mul_i<SG>(make_float2(s2 * m1.x - s1 * m2.x, s2 * m1.y - s1 * m2.y));
  a0 = cadd(a0, cadd(p1, p2));
  a1 = cadd(t1, u1); a4 = csub(t1, u1);
  a2 = cadd(t2, u2); a3 = csub(t2, u2);
}
template <int SG> __device__ __forceinline__ void dft10(float2* v) {
  float2 e0 = v[0], e1 = v[2], e2 = v[4], e3 = v[6], e4 = v[8];
  float2 o0 = v[1], o1 = v[3], o2 = v[5], o3 = v[7], o4 = v[9];
  dft5<SG>(e0, e1, e2, e3, e4); dft5<SG>(o0, o1, o2, o3, o4);
  const float c1 = 0.80901699437494742410f, s1 = 0.58778525229247312917f;
  const float c2 = 0.30901699437494742410f, s2 = 0.95105651629515357212f;
  o1 = cmul(o1, make_float2(c1, SG * s1));
  o2 = cmul(o2, make_float2(c2, SG * s2));
  o3 = cmul(o3, make_float2(-c2, SG * s2));
  o4 = cmul(o4, make_float2(-c1, SG * s1));
  v[0] = cadd(e0, o0); v[5] = csub(e0, o0);
  v[1] = cadd(e1, o1); v[6] = csub(e1, o1);
  v[2] = cadd(e2, o2); v[7] = csub(e2, o2);
  v[3] = cadd(e3, o3); v[8] = csub(e3, o3);
  v[4] = cadd(e4, o4); v[9] = csub(e4, o4);
}

__device__ __forceinline__ int PD(int i) { return i + (i >> 3); }
__device__ __forceinline__ int swz(int row) { return (-(row >> 2)) & 3; }
__device__ __forceinline__ size_t kb_off(int R, int row, int k) {
  return (size_t)(k >> 5) * ((size_t)R * 32) + (size_t)row * 32 + ((((k >> 3) & 3) ^ swz(row)) << 3) + (k & 7);
}

template <int SG> __device__ __forceinline__ float2 cispi(float x) {
  float sn, cs; sincospif(x, &sn, &cs);
  return make_float2(cs, SG * sn);
}
template <int R> __device__ __forceinline__ void twiddle_pow(float2* v, float2 w1) {
  float2 w2 = cmul(w1, w1), w3 = cmul(w2, w1), w4 = cmul(w2, w2);
  v[1] = cmul(v[1], w1); v[2] = cmul(v[2], w2); v[3] = cmul(v[3], w3);
  if (R > 4) {
    float2 w5 = cmul(w4, w1), w6 = cmul(w3, w3), w7 = cmul(w4, w3);
    v[4] = cmul(v[4], w4); v[5] = cmul(v[5], w5); v[6] = cmul(v[6], w6); v[7] = cmul(v[7], w7);
    if (R > 8) { float2 w8 = cmul(w4, w4), w9 = cmul(w8, w1); v[8] = cmul(v[8], w8); v[9] = cmul(v[9], w9); }
  }
}

template <int SG>
__device__ __forceinline__ void fft640(float2* b0, float2* b1, int j, float2 w2_, float2 w3_) {
  float2 v[10];
#pragma unroll
  for (int r = 0; r < 8; ++r) v[r] = b0[PD(j + 80 * r)];
  dft8<SG>(v);
#pragma unroll
  for (int r = 0; r < 8; ++r) b1[PD(8 * j + r)] = v[r];
  __syncthreads();
  {
    int m = j & 7;
#pragma unroll
    for (int r = 0; r < 8; ++r) v[r] = b1[PD(j + 80 * r)];
    twiddle_pow<8>(v, w2_);
    dft8<SG>(v);
#pragma unroll
    for (int r = 0; r < 8; ++r) b0[PD((j >> 3) * 64 + m + 8 * r)] = v[r];
  }
  __syncthreads();
  if (j < 64) {
#pragma unroll
    for (int r = 0; r < 10; ++r) v[r] = b0[PD(j + 64 * r)];
    twiddle_pow<10>(v, w3_);
    dft10<SG>(v);
#pragma unroll
    for (int r = 0; r < 10; ++r) b1[PD(j + 64 * r)] = v[r];
  }
}

__device__ __forceinline__ void ln16(float* xv, float* red, int row, int c) {
  float s = 0.f;
#pragma unroll
  for (int i = 0; i < 16; ++i) s += xv[i];
  s += __shfl_xor(s, 1); s += __shfl_xor(s, 2); s += __shfl_xor(s, 4);
  if ((c & 7) == 0) red[row * 5 + (c >> 3)] = s;
  __syncthreads();
  float mean = (red[row * 5] + red[row * 5 + 1] + red[row * 5 + 2] + red[row * 5 + 3] + red[row * 5 + 4]) * (1.0f / 640.0f);
  float q = 0.f;
#pragma unroll
  for (int i = 0; i < 16; ++i) { float d = xv[i] - mean; q += d * d; }
  q += __shfl_xor(q, 1); q += __shfl_xor(q, 2); q += __shfl_xor(q, 4);
  if ((c & 7) == 0) red[40 + row * 5 + (c >> 3)] = q;
  __syncthreads();
  float var = (red[40 + row * 5] + red[40 + row * 5 + 1] + red[40 + row * 5 + 2] + red[40 + row * 5 + 3] + red[40 + row * 5 + 4]) * (1.0f / 640.0f);
  float mb = rbf(mean), vb = rbf(var);
  float den = rbf(sqrtf(rbf(vb + 1e-5f)));
#pragma unroll
  for (int i = 0; i < 16; ++i) xv[i] = rbf(rbf(xv[i] - mb) / den);
}

constexpr int FB = 720;

__global__ __launch_bounds__(320) void k_a1(const u16* __restrict__ x, float2* __restrict__ Zt,
                                            const u16* __restrict__ wr, const u16* __restrict__ wi,
                                            unsigned* __restrict__ Wt, int do_prep) {
  __shared__ float2 buf[2][4][FB];
  __shared__ float red[80];
  const int t = threadIdx.x, s0 = blockIdx.x * 8;
  if (do_prep) {
    for (int k = t; k < DF_; k += 320) {
      unsigned p[8];
#pragma unroll
      for (int r = 0; r < 8; ++r)
        p[r] = (unsigned)wr[(s0 + r) * DF_ + k] | ((unsigned)wi[(s0 + r) * DF_ + k] << 16);
      uint4* dst = (uint4*)(Wt + (size_t)k * S_ + s0);
      dst[0] = make_uint4(p[0], p[1], p[2], p[3]);
      dst[1] = make_uint4(p[4], p[5], p[6], p[7]);
    }
  }
  const int row = t / 40, c = t % 40;
  float xv[16];
  {
    const uint4* src = (const uint4*)(x + (size_t)(s0 + row) * D_ + c * 16);
    uint4 a = src[0], b = src[1];
    unsigned u[8] = {a.x, a.y, a.z, a.w, b.x, b.y, b.z, b.w};
#pragma unroll
    for (int i = 0; i < 8; ++i) { xv[2 * i] = __uint_as_float(u[i] << 16); xv[2 * i + 1] = __uint_as_float(u[i] & 0xffff0000u); }
  }
  const int f = t / 80, j = t % 80;
  const float2 w2_ = cispi<-1>((float)(j & 7) * (1.0f / 32.0f)), w3_ = cispi<-1>((float)j * (1.0f / 320.0f));
  ln16(xv, red, row, c);
  {
    float* dst = (float*)&buf[0][row >> 1][0] + (row & 1);
#pragma unroll
    for (int i = 0; i < 16; ++i) dst[2 * PD(c * 16 + i)] = xv[i];
  }
  __syncthreads();
  fft640<-1>(buf[0][f], buf[1][f], j, w2_, w3_);
  __syncthreads();
  for (int e = t; e < DF_ * 4; e += 320) {
    int k = e >> 2, ff = e & 3;
    float2 a = buf[1][ff][PD(k)], b = buf[1][ff][PD(k == 0 ? 0 : 640 - k)];
    float4 o;
    o.x = 0.5f * (a.x + b.x); o.y = 0.5f * (a.y - b.y);
    o.z = 0.5f * (a.y + b.y); o.w = 0.5f * (b.x - a.x);
    *(float4*)(Zt + (size_t)k * S_ + s0 + 2 * ff) = o;
  }
}

__device__ __forceinline__ void pack_w13(const u16* __restrict__ src, u16* __restrict__ dst, int which, int nb, int k0, u16* sm) {
  const int t = threadIdx.x;
  for (int e = t; e < 64 * 27; e += 256) {
    int r = e / 27, q = e % 27;
    uint2 v = *(const uint2*)(src + (size_t)(k0 + r) * F_ + nb * 108 + q * 4);
    sm[(q * 4 + 0) * 66 + r] = (u16)(v.x & 0xffff); sm[(q * 4 + 1) * 66 + r] = (u16)(v.x >> 16);
    sm[(q * 4 + 2) * 66 + r] = (u16)(v.y & 0xffff); sm[(q * 4 + 3) * 66 + r] = (u16)(v.y >> 16);
  }
  __syncthreads();
  for (int e = t; e < 112 * 8; e += 256) {
    int c = e >> 3, q = e & 7;
    uint4 v = make_uint4(0, 0, 0, 0);
    if (c < 108) {
      u16* pp = (u16*)&v;
#pragma unroll
      for (int i = 0; i < 8; ++i) pp[i] = sm[c * 66 + q * 8 + i];
    }
    int drow = nb * 224 + (c >> 4) * 32 + which * 16 + (c & 15);
    *(uint4*)(dst + kb_off(16 * 224, drow, k0 + q * 8)) = v;
  }
}
__device__ __forceinline__ void pack_w2(const u16* __restrict__ src, u16* __restrict__ dst, int nb, int d0, u16* sm) {
  const int t = threadIdx.x;
  for (int e = t; e < 108 * 8; e += 256) {
    int i = e >> 3, q = e & 7;
    uint4 v = *(const uint4*)(src + (size_t)(nb * 108 + i) * D_ + d0 + q * 8);
    u16* pp = (u16*)&v;
#pragma unroll
    for (int j = 0; j < 8; ++j) sm[(q * 8 + j) * 114 + i] = pp[j];
  }
  __syncthreads();
  for (int e = t; e < 64 * 14; e += 256) {
    int d = e / 14, q = e % 14;
    uint4 v; u16* pp = (u16*)&v;
#pragma unroll
    for (int j = 0; j < 8; ++j) { int i = q * 8 + j; pp[j] = i < 108 ? sm[d * 114 + i] : (u16)0; }
    *(uint4*)(dst + kb_off(D_, d0 + d, nb * 112 + q * 8)) = v;
  }
}

constexpr int CB = 2048 + 256;
__global__ __launch_bounds__(256) void k_a2(float2* __restrict__ Zt, const unsigned* __restrict__ Wt,
                                            const u16* __restrict__ w1, const u16* __restrict__ w3, const u16* __restrict__ w2,
                                            u16* __restrict__ w13p, u16* __restrict__ w2t) {
  __shared__ float2 smem[2 * CB];
  const int t = threadIdx.x;
  if (blockIdx.x >= DF_) {
    int id = blockIdx.x - DF_;
    u16* sm = (u16*)smem;
    if (id < 320) {
      int which = id / 160, r = id % 160;
      pack_w13(which ? w3 : w1, w13p, which, r / 10, (r % 10) * 64, sm);
    } else {
      int r = id - 320;
      pack_w2(w2, w2t, r / 10, (r % 10) * 64, sm);
    }
    return;
  }
  float2* b0 = smem; float2* b1 = smem + CB;
  const int k = blockIdx.x;
  float2* col = Zt + (size_t)k * S_;
  float2 v[8];
#pragma unroll
  for (int r = 0; r < 8; ++r) v[r] = col[t + 256 * r];
  unsigned wv[8];
#pragma unroll
  for (int r = 0; r < 8; ++r) wv[r] = Wt[(size_t)k * S_ + t + 256 * r];
  const float2 wp2 = cispi<-1>((float)(t & 7) * (1.0f / 32.0f));
  const float2 wp3 = cispi<-1>((float)(t & 63) * (1.0f / 256.0f));
  const float2 wp4a = cispi<-1>((float)t * (1.0f / 1024.0f)), wp4b = cispi<-1>((float)(t + 256) * (1.0f / 1024.0f));
  dft8<-1>(v);
#pragma unroll
  for (int r = 0; r < 8; ++r) b0[PD(8 * t + r)] = v[r];
  __syncthreads();
  {
    int m = t & 7;
#pragma unroll
    for (int r = 0; r < 8; ++r) v[r] = b0[PD(t + 256 * r)];
    twiddle_pow<8>(v, wp2);
    dft8<-1>(v);
#pragma unroll
    for (int r = 0; r < 8; ++r) b1[PD((t >> 3) * 64 + m + 8 * r)] = v[r];
  }
  __syncthreads();
  {
    int m = t & 63;
#pragma unroll
    for (int r = 0; r < 8; ++r) v[r] = b1[PD(t + 256 * r)];
    twiddle_pow<8>(v, wp3);
    dft8<-1>(v);
#pragma unroll
    for (int r = 0; r < 8; ++r) b0[PD((t >> 6) * 512 + m + 64 * r)] = v[r];
  }
  __syncthreads();
#pragma unroll
  for (int h = 0; h < 2; ++h) {
    int j = t + 256 * h;
    float2 a[4] = {b0[PD(j)], b0[PD(j + 512)], b0[PD(j + 1024)], b0[PD(j + 1536)]};
    twiddle_pow<4>(a, h ? wp4b : wp4a);
    dft4<-1>(a[0], a[1], a[2], a[3]);
    v[h] = a[0]; v[h + 2] = a[1]; v[h + 4] = a[2]; v[h + 6] = a[3];
  }
  const float sc = 1.0f / (2048.0f * 640.0f);
#pragma unroll
  for (int r = 0; r < 8; ++r) {
    float2 w = make_float2(__uint_as_float(wv[r] << 16) * sc, __uint_as_float(wv[r] & 0xffff0000u) * sc);
    v[r] = cmul(v[r], w);
  }
  dft8<1>(v);
#pragma unroll
  for (int r = 0; r < 8; ++r) b1[PD(8 * t + r)] = v[r];
  __syncthreads();
  {
    int m = t & 7;
#pragma unroll
    for (int r = 0; r < 8; ++r) v[r] = b1[PD(t + 256 * r)];
    twiddle_pow<8>(v, twc<1>(wp2));
    dft8<1>(v);
#pragma unroll
    for (int r = 0; r < 8; ++r) b0[PD((t >> 3) * 64 + m + 8 * r)] = v[r];
  }
  __syncthreads();
  {
    int m = t & 63;
#pragma unroll
    for (int r = 0; r < 8; ++r) v[r] = b0[PD(t + 256 * r)];
    twiddle_pow<8>(v, twc<1>(wp3));
    dft8<1>(v);
#pragma unroll
    for (int r = 0; r < 8; ++r) b1[PD((t >> 6) * 512 + m + 64 * r)] = v[r];
  }
  __syncthreads();
#pragma unroll
  for (int h = 0; h < 2; ++h) {
    int j = t + 256 * h;
    float2 a[4] = {b1[PD(j)], b1[PD(j + 512)], b1[PD(j + 1024)], b1[PD(j + 1536)]};
    twiddle_pow<4>(a, twc<1>(h ? wp4b : wp4a));
    dft4<1>(a[0], a[1], a[2], a[3]);
    col[j] = a[0]; col[j + 512] = a[1]; col[j + 1024] = a[2]; col[j + 1536] = a[3];
  }
}

__global__ __launch_bounds__(320) void k_a3(const float2* __restrict__ Zt, const u16* xin,
                                            u16* xout, u16* __restrict__ xln) {
  __shared__ float2 buf[2][4][FB];
  __shared__ float red[80];
  const int t = threadIdx.x, s0 = blockIdx.x * 8;
  float4 z[5];
#pragma unroll
  for (int i = 0; i < 5; ++i) {
    int e = t + 320 * i;
    if (i < 4 || e < DF_ * 4) z[i] = *(const float4*)(Zt + (size_t)(e >> 2) * S_ + s0 + 2 * (e & 3));
  }
  const int row = t / 40, c = t % 40;
  float xv[16];
  {
    const uint4* src = (const uint4*)(xin + (size_t)(s0 + row) * D_ + c * 16);
    uint4 a = src[0], b = src[1];
    unsigned u[8] = {a.x, a.y, a.z, a.w, b.x, b.y, b.z, b.w};
#pragma unroll
    for (int i = 0; i < 8; ++i) { xv[2 * i] = __uint_as_float(u[i] << 16); xv[2 * i + 1] = __uint_as_float(u[i] & 0xffff0000u); }
  }
  const int f = t / 80, j = t % 80;
  const float2 w2_ = cispi<1>((float)(j & 7) * (1.0f / 32.0f)), w3_ = cispi<1>((float)j * (1.0f / 320.0f));
#pragma unroll
  for (int i = 0; i < 5; ++i) {
    int e = t + 320 * i;
    if (i < 4 || e < DF_ * 4) {
      int k = e >> 2, ff = e & 3;
      if (k == 0 || k == 320) {
        buf[0][ff][PD(k)] = make_float2(z[i].x, z[i].z);
      } else {
        buf[0][ff][PD(k)] = make_float2(z[i].x - z[i].w, z[i].y + z[i].z);
        buf[0][ff][PD(640 - k)] = make_float2(z[i].x + z[i].w, z[i].z - z[i].y);
      }
    }
  }
  __syncthreads();
  fft640<1>(buf[0][f], buf[1][f], j, w2_, w3_);
  __syncthreads();
  {
    const float* src = (const float*)&buf[1][row >> 1][0] + (row & 1);
    unsigned u[8];
#pragma unroll
    for (int i = 0; i < 16; ++i) xv[i] = rbf(xv[i] + rbf(src[2 * PD(c * 16 + i)]));
#pragma unroll
    for (int i = 0; i < 8; ++i) u[i] = (__float_as_uint(xv[2 * i]) >> 16) | (__float_as_uint(xv[2 * i + 1]) & 0xffff0000u);
    uint4* dst = (uint4*)(xout + (size_t)(s0 + row) * D_ + c * 16);
    dst[0] = make_uint4(u[0], u[1], u[2], u[3]); dst[1] = make_uint4(u[4], u[5], u[6], u[7]);
  }
  ln16(xv, red, row, c);
  {
    unsigned u[8];
#pragma unroll
    for (int i = 0; i < 8; ++i) u[i] = (__float_as_uint(xv[2 * i]) >> 16) | (__float_as_uint(xv[2 * i + 1]) & 0xffff0000u);
    *(uint4*)(xln + kb_off(S_, s0 + row, c * 16)) = make_uint4(u[0], u[1], u[2], u[3]);
    *(uint4*)(xln + kb_off(S_, s0 + row, c * 16 + 8)) = make_uint4(u[4], u[5], u[6], u[7]);
  }
}

template <int N> __device__ __forceinline__ void wait_vm() { asm volatile("s_waitcnt vmcnt(%0)" ::"i"(N) : "memory"); }
__device__ __forceinline__ void wait_lgkm0() { asm volatile("s_waitcnt lgkmcnt(0)" ::: "memory"); }

#define STAMP_DECL
#define STAMP_AT()
#define STAMP_OUT(w)
constexpr int FP_ = 1792;

template <int NP, int L>
__device__ __forceinline__ void g1_body(const u16* __restrict__ A, const u16* __restrict__ Bt, u16* __restrict__ out,
                                        char* lds, int bm, int bn, int wm, int p0) {
  constexpr int BM = 128, BN = 224, NS = 6, NT = 20, PF = 5;
  constexpr int ABYTES = BM * 64, BBYTES = BN * 64, STG = ABYTES + BBYTES;
  const int t = threadIdx.x, lane = t & 63, fr = lane & 15, fq = lane >> 4;
  const char* Ag = (const char*)A + (size_t)bm * ABYTES + t * 16;
  const char* Bg = (const char*)Bt + (size_t)bn * BBYTES + t * 16;
  auto stage = [&](int ks) {
    char* la = lds + (ks % NS) * STG + t * 16;
    __builtin_amdgcn_global_load_lds((const unsigned*)(Ag + (size_t)ks * (S_ * 64)), (unsigned*)la, 16, 0, 0);
    __builtin_amdgcn_global_load_lds((const unsigned*)(Bg + (size_t)ks * (3584 * 64)), (unsigned*)(la + ABYTES), 16, 0, 0);
    if (L == 3)
      __builtin_amdgcn_global_load_lds((const unsigned*)(Bg + (size_t)ks * (3584 * 64) + 8192), (unsigned*)(la + ABYTES + 8192), 16, 0, 0);
  };
  const int aoff = (wm * 32 + fr) * 64 + ((fq ^ swz(fr)) << 4);
  const int boff = ABYTES + (p0 * 32 + fr) * 64 + ((fq ^ swz(fr)) << 4);
  bf16x8 af[2][2], bfr[2][2 * NP];
  auto ldfr = [&](int set, int ks) {
    const char* l = lds + (ks % NS) * STG;
#pragma unroll
    for (int m = 0; m < 2; ++m) af[set][m] = *(const bf16x8*)(l + aoff + m * 1024);
#pragma unroll
    for (int n = 0; n < 2 * NP; ++n) bfr[set][n] = *(const bf16x8*)(l + boff + n * 1024);
  };
  f32x4 acc[2][2 * NP];
#pragma unroll
  for (int m = 0; m < 2; ++m)
#pragma unroll
    for (int n = 0; n < 2 * NP; ++n) acc[m][n] = (f32x4){0.f, 0.f, 0.f, 0.f};
  STAMP_DECL STAMP_AT();
#pragma unroll
  for (int i = 0; i < PF; ++i) stage(i);
  wait_vm<(PF - 1) * L>();
  __builtin_amdgcn_s_barrier();
  ldfr(0, 0);
#pragma unroll
  for (int ks = 0; ks < NT; ++ks) {
    STAMP_AT();
    const int cur = ks & 1;
    if (ks + 1 < NT) {
      constexpr int dummy = 0; (void)dummy;
      const int issued = (ks + PF - 1 < NT - 1) ? ks + PF - 1 : NT - 1;
      const int allow = (issued - (ks + 1)) * L;
      if (allow >= 3 * L) wait_vm<3 * L>(); else if (allow == 2 * L) wait_vm<2 * L>(); else if (allow == L) wait_vm<L>(); else wait_vm<0>();
      __builtin_amdgcn_s_barrier();
      if (ks + PF < NT) stage(ks + PF);
      ldfr(cur ^ 1, ks + 1);
    }
#pragma unroll
    for (int n = 0; n < 2 * NP; ++n)
#pragma unroll
      for (int m = 0; m < 2; ++m)
        acc[m][n] = __builtin_amdgcn_mfma_f32_16x16x32_bf16(bfr[cur][n], af[cur][m], acc[m][n], 0, 0, 0);
    if (ks + 1 < NT) {
#pragma unroll
      for (int g = 0; g < L; ++g) {
        __builtin_amdgcn_sched_group_barrier(0x8, 1, 0);
        __builtin_amdgcn_sched_group_barrier(0x10, 1, 0);
      }
#pragma unroll
      for (int g = 0; g < 2 * NP + 2; ++g) {
        __builtin_amdgcn_sched_group_barrier(0x8, 1, 0);
        __builtin_amdgcn_sched_group_barrier(0x100, 1, 0);
      }
    }
    __builtin_amdgcn_sched_barrier(0);
  }
  STAMP_AT();
#pragma unroll
  for (int m = 0; m < 2; ++m) {
    int row = bm * BM + wm * 32 + m * 16 + fr;
#pragma unroll
    for (int p = 0; p < NP; ++p) {
      int hc = bn * 112 + (p0 + p) * 16 + fq * 4;
      u16 o[4];
#pragma unroll
      for (int j = 0; j < 4; ++j) {
        float g = rbf(acc[m][2 * p][j]), u = rbf(acc[m][2 * p + 1][j]);
        float sg = rbf(g * __builtin_amdgcn_rcpf(1.0f + __expf(-g)));
        o[j] = f2bf(sg * u);
      }
      *(uint2*)(out + kb_off(S_, row, hc)) = make_uint2(o[0] | ((unsigned)o[1] << 16), o[2] | ((unsigned)o[3] << 16));
    }
  }
  STAMP_AT();
  STAMP_OUT(0);
}
__global__ __launch_bounds__(512) void k_g1(const u16* __restrict__ A, const u16* __restrict__ Bt, u16* __restrict__ out) {
  __shared__ __attribute__((aligned(16))) char lds[6 * (128 + 224) * 64];
  const int wid = __builtin_amdgcn_readfirstlane(threadIdx.x >> 6);
  const int xcd = blockIdx.x & 7, idx = blockIdx.x >> 3;
  const int bm = (xcd >> 1) * 4 + (idx & 3), bn = (xcd & 1) * 8 + (idx >> 2);
  if (wid < 4) g1_body<4, 3>(A, Bt, out, lds, bm, bn, wid, 0);
  else if (wid < 6) g1_body<3, 3>(A, Bt, out, lds, bm, bn, wid - 4, 4);
  else g1_body<3, 2>(A, Bt, out, lds, bm, bn, wid - 4, 4);
}

template <int L>
__device__ __forceinline__ void g2_body(const u16* __restrict__ A, const u16* __restrict__ Bt, const u16* res, u16* out,
                                        char* lds, int bm, int bn, int wid) {
  constexpr int BM = 64, BN = 80, NS = 4, NT = 14, PF = 3;
  constexpr int ABYTES = 4 * BM * 64, BBYTES = 4 * BN * 64, STG = ABYTES + BBYTES;
  const int t = threadIdx.x, lane = t & 63, fr = lane & 15, fq = lane >> 4;
  const int ks4 = wid & 3, mh = wid >> 2;
  const char* Ag[2]; const char* Bg[3];
#pragma unroll
  for (int i = 0; i < 2; ++i) { int by = t * 16 + i * 8192; Ag[i] = (const char*)A + (size_t)(by >> 12) * (S_ * 64) + (size_t)bm * 4096 + (by & 4095); }
#pragma unroll
  for (int i = 0; i < 3; ++i) { int by = t * 16 + i * 8192; int kb = by / 5120; Bg[i] = (const char*)Bt + (size_t)kb * (D_ * 64) + (size_t)bn * 5120 + (by - kb * 5120); }
  auto stage = [&](int st) {
    char* la = lds + (st % NS) * STG + t * 16;
#pragma unroll
    for (int i = 0; i < 2; ++i)
      __builtin_amdgcn_global_load_lds((const unsigned*)(Ag[i] + (size_t)st * (4 * S_ * 64)), (unsigned*)(la + i * 8192), 16, 0, 0);
#pragma unroll
    for (int i = 0; i < L - 2; ++i)
      __builtin_amdgcn_global_load_lds((const unsigned*)(Bg[i] + (size_t)st * (4 * D_ * 64)), (unsigned*)(la + ABYTES + i * 8192), 16, 0, 0);
  };
  const int aoff = ks4 * 4096 + (mh * 32 + fr) * 64 + ((fq ^ swz(fr)) << 4);
  const int boff = ABYTES + ks4 * 5120 + fr * 64 + ((fq ^ swz(fr)) << 4);
  bf16x8 af[2][2], bfr[2][5];
  auto ldfr = [&](int set, int st) {
    const char* l = lds + (st % NS) * STG;
#pragma unroll
    for (int m = 0; m < 2; ++m) af[set][m] = *(const bf16x8*)(l + aoff + m * 1024);
#pragma unroll
    for (int n = 0; n < 5; ++n) bfr[set][n] = *(const bf16x8*)(l + boff + n * 1024);
  };
  f32x4 acc[2][5];
#pragma unroll
  for (int m = 0; m < 2; ++m)
#pragma unroll
    for (int n = 0; n < 5; ++n) acc[m][n] = (f32x4){0.f, 0.f, 0.f, 0.f};
  STAMP_DECL STAMP_AT();
  uint2 rvp[3];
#pragma unroll
  for (int i = 0; i < 3; ++i) {
    int fi = wid + 8 * i; if (fi >= 20) fi = wid;
    rvp[i] = *(const uint2*)(res + (size_t)(bm * BM + (fi / 5) * 16 + fr) * D_ + bn * BN + (fi % 5) * 16 + fq * 4);
  }
#pragma unroll
  for (int i = 0; i < PF; ++i) stage(i);
  wait_vm<(PF - 1) * L>();
  __builtin_amdgcn_s_barrier();
  ldfr(0, 0);
#pragma unroll
  for (int st = 0; st < NT; ++st) {
    STAMP_AT();
    const int cur = st & 1;
    if (st + 1 < NT) {
      const int issued = (st + PF - 1 < NT - 1) ? st + PF - 1 : NT - 1;
      const int allow = (issued - (st + 1)) * L;
      if (allow >= L) wait_vm<L>(); else wait_vm<0>();
      __builtin_amdgcn_s_barrier();
      if (st + PF < NT) stage(st + PF);
      ldfr(cur ^ 1, st + 1);
    }
#pragma unroll
    for (int m = 0; m < 2; ++m)
#pragma unroll
      for (int n = 0; n < 5; ++n)
        acc[m][n] = __builtin_amdgcn_mfma_f32_16x16x32_bf16(bfr[cur][n], af[cur][m], acc[m][n], 0, 0, 0);
    if (st + 1 < NT) {
#pragma unroll
      for (int g = 0; g < 5; ++g) {
        __builtin_amdgcn_sched_group_barrier(0x8, 1, 0);
        __builtin_amdgcn_sched_group_barrier(0x10, 1, 0);
        __builtin_amdgcn_sched_group_barrier(0x100, 1, 0);
      }
#pragma unroll
      for (int g = 0; g < 2; ++g) {
        __builtin_amdgcn_sched_group_barrier(0x8, 1, 0);
        __builtin_amdgcn_sched_group_barrier(0x100, 1, 0);
      }
    }
    __builtin_amdgcn_sched_barrier(0);
  }
  STAMP_AT();
  __syncthreads();
  f32x4* red = (f32x4*)lds;
#pragma unroll
  for (int m = 0; m < 2; ++m)
#pragma unroll
    for (int n = 0; n < 5; ++n) red[(ks4 * 20 + mh * 10 + m * 5 + n) * 64 + lane] = acc[m][n];
  __syncthreads();
#pragma unroll
  for (int i = 0; i < 3; ++i) {
    int fi = wid + 8 * i;
    if (fi < 20) {
      f32x4 s = red[fi * 64 + lane] + red[(20 + fi) * 64 + lane] + red[(40 + fi) * 64 + lane] + red[(60 + fi) * 64 + lane];
      int mrow = fi / 5, n = fi % 5;
      int row = bm * BM + mrow * 16 + fr;
      int cc = bn * BN + n * 16 + fq * 4;
      uint2 rv = rvp[i];
      float r0 = __uint_as_float(rv.x << 16), r1 = __uint_as_float(rv.x & 0xffff0000u);
      float r2 = __uint_as_float(rv.y << 16), r3 = __uint_as_float(rv.y & 0xffff0000u);
      u16 o0 = f2bf(r0 + rbf(s[0])), o1 = f2bf(r1 + rbf(s[1]));
      u16 o2 = f2bf(r2 + rbf(s[2])), o3 = f2bf(r3 + rbf(s[3]));
      *(uint2*)(out + (size_t)row * D_ + cc) = make_uint2(o0 | ((unsigned)o1 << 16), o2 | ((unsigned)o3 << 16));
    }
  }
  STAMP_AT();
  STAMP_OUT(1);
}
__global__ __launch_bounds__(512) void k_g2(const u16* __restrict__ A, const u16* __restrict__ Bt, const u16* res, u16* out) {
  __shared__ __attribute__((aligned(16))) char lds[4 * 4 * (64 + 80) * 64];
  const int wid = __builtin_amdgcn_readfirstlane(threadIdx.x >> 6);
  const int xcd = blockIdx.x & 7, idx = blockIdx.x >> 3;
  const int bm = xcd * 4 + (idx & 3), bn = idx >> 2;
  if (wid < 4) g2_body<5>(A, Bt, res, out, lds, bm, bn, wid);
  else g2_body<4>(A, Bt, res, out, lds, bm, bn, wid);
}

extern "C" void kernel_launch(void* const* d_in, const int* in_sizes, int n_in,
                              void* d_out, int out_size, void* d_ws, size_t ws_size,
                              hipStream_t stream) {
  const u16* Ax = (const u16*)d_in[0];
  const u16* wr = (const u16*)d_in[1];
  const u16* wi = (const u16*)d_in[2];
  const u16* w1 = (const u16*)d_in[4];
  const u16* w3 = (const u16*)d_in[5];
  const u16* w2 = (const u16*)d_in[6];
  char* ws = (char*)d_ws;
  size_t off = 0;
  auto alloc = [&](size_t bytes) { void* p = ws + off; off += (bytes + 255) & ~(size_t)255; return p; };
  float2* Zt = (float2*)alloc((size_t)DF_ * S_ * 8);
  unsigned* Wt = (unsigned*)alloc((size_t)DF_ * S_ * 4);
  u16* xb = (u16*)alloc((size_t)S_ * D_ * 2);
  u16* xln = (u16*)alloc((size_t)S_ * D_ * 2);
  u16* h = (u16*)alloc((size_t)S_ * 1792 * 2);
  u16* w13p = (u16*)alloc((size_t)16 * 224 * D_ * 2);
  u16* w2t = (u16*)alloc((size_t)D_ * 1792 * 2);
  u16* outp = (u16*)d_out;

  for (int layer = 0; layer < 2; ++layer) {
    const u16* xin = layer == 0 ? Ax : xb;
    k_a1<<<256, 320, 0, stream>>>(xin, Zt, wr, wi, Wt, layer == 0 ? 1 : 0);
    k_a2<<<layer == 0 ? DF_ + 480 : DF_, 256, 0, stream>>>(Zt, Wt, w1, w3, w2, w13p, w2t);
    k_a3<<<256, 320, 0, stream>>>(Zt, xin, xb, xln);
    k_g1<<<256, 512, 0, stream>>>(xln, w13p, h);
    k_g2<<<256, 512, 0, stream>>>(h, w2t, xb, layer == 0 ? xb : outp);
  }
}
```

```cpp
#include <hip/hip_runtime.h>
#include <hip/hip_bf16.h>
#include <stdint.h>

typedef unsigned short u16;
using bf16x8 = __attribute__((ext_vector_type(8))) short;
using f32x4 = __attribute__((ext_vector_type(4))) float;

constexpr int S_ = 2048, D_ = 640, DF_ = 321, F_ = 1728;

__device__ __forceinline__ float bf2f(u16 v) { return __uint_as_float(((unsigned)v) << 16); }
__device__ __forceinline__ u16 f2bf(float f) { return __bfloat16_as_ushort(__float2bfloat16(f)); }
__device__ __forceinline__ float rbf(float f) { return bf2f(f2bf(f)); }

__device__ __forceinline__ float2 cadd(float2 a, float2 b) { return make_float2(a.x + b.x, a.y + b.y); }
__device__ __forceinline__ float2 csub(float2 a, float2 b) { return make_float2(a.x - b.x, a.y - b.y); }
__device__ __forceinline__ float2 cmul(float2 a, float2 b) {
  return make_float2(a.x * b.x - a.y * b.y, a.x * b.y + a.y * b.x);
}
template <int SG> __device__ __forceinline__ float2 mul_i(float2 a) {
  return SG > 0 ? make_float2(-a.y, a.x) : make_float2(a.y, -a.x);
}
template <int SG> __device__ __forceinline__ float2 twc(float2 w) {
  return SG > 0 ? make_float2(w.x, -w.y) : w;
}

template <int SG> __device__ __forceinline__ void dft4(float2& a0, float2& a1, float2& a2, float2& a3) {
  float2 s0 = cadd(a0, a2), d0 = csub(a0, a2), s1 = cadd(a1, a3), d1 = mul_i<SG>(csub(a1, a3));
  a0 = cadd(s0, s1); a2 = csub(s0, s1); a1 = cadd(d0, d1); a3 = csub(d0, d1);
}
template <int SG> __device__ __forceinline__ void dft8(float2* v) {
  float2 e0 = v[0], e1 = v[2], e2 = v[4], e3 = v[6], o0 = v[1], o1 = v[3], o2 = v[5], o3 = v[7];
  dft4<SG>(e0, e1, e2, e3); dft4<SG>(o0, o1, o2, o3);
  const float h = 0.70710678118654752440f;
  float2 t1 = make_float2(h * (o1.x - SG * o1.y), h * (o1.y + SG * o1.x));
  float2 t2 = mul_i<SG>(o2);
  float2 t3 = make_float2(h * (-o3.x - SG * o3.y), h * (-o3.y + SG * o3.x));
  v[0] = cadd(e0, o0); v[4] = csub(e0, o0);
  v[1] = cadd(e1, t1); v[5] = csub(e1, t1);
  v[2] = cadd(e2, t2); v[6] = csub(e2, t2);
  v[3] = cadd(e3, t3); v[7] = csub(e3, t3);
}
template <int SG> __device__ __forceinline__ void dft5(float2& a0, float2& a1, float2& a2, float2& a3, float2& a4) {
  const float c1 = 0.30901699437494742410f, c2 = -0.80901699437494742410f;
  const float s1 = 0.95105651629515357212f, s2 = 0.58778525229247312917f;
  float2 p1 = cadd(a1, a4), m1 = csub(a1, a4), p2 = cadd(a2, a3), m2 = csub(a2, a3);
  float2 t1 = make_float2(a0.x + c1 * p1.x + c2 * p2.x, a0.y + c1 * p1.y + c2 * p2.y);
  float2 t2 = make_float2(a0.x + c2 * p1.x + c1 * p2.x, a0.y + c2 * p1.y + c1 * p2.y);
  float2 u1 = mul_i<SG>(make_float2(s1 * m1.x + s2 * m2.x, s1 * m1.y + s2 * m2.y));
  float2 u2 = mul_i<SG>(make_float2(s2 * m1.x - s1 * m2.x, s2 * m1.y - s1 * m2.y));
  a0 = cadd(a0, cadd(p1, p2));
  a1 = cadd(t1, u1); a4 = csub(t1, u1);
  a2 = cadd(t2, u2); a3 = csub(t2, u2);
}
template <int SG> __device__ __forceinline__ void dft10(float2* v) {
  float2 e0 = v[0], e1 = v[2], e2 = v[4], e3 = v[6], e4 = v[8];
  float2 o0 = v[1], o1 = v[3], o2 = v[5], o3 = v[7], o4 = v[9];
  dft5<SG>(e0, e1, e2, e3, e4); dft5<SG>(o0, o1, o2, o3, o4);
  const float c1 = 0.80901699437494742410f, s1 = 0.58778525229247312917f;
  const float c2 = 0.30901699437494742410f, s2 = 0.95105651629515357212f;
  o1 = cmul(o1, make_float2(c1, SG * s1));
  o2 = cmul(o2, make_float2(c2, SG * s2));
  o3 = cmul(o3, make_float2(-c2, SG * s2));
  o4 = cmul(o4, make_float2(-c1, SG * s1));
  v[0] = cadd(e0, o0); v[5] = csub(e0, o0);
  v[1] = cadd(e1, o1); v[6] = csub(e1, o1);
  v[2] = cadd(e2, o2); v[7] = csub(e2, o2);
  v[3] = cadd(e3, o3); v[8] = csub(e3, o3);
  v[4] = cadd(e4, o4); v[9] = csub(e4, o4);
}

__device__ __forceinline__ int PD(int i) { return i + (i >> 3); }
__device__ __forceinline__ int swz(int row) { return (-(row >> 2)) & 3; }
__device__ __forceinline__ size_t kb_off(int R, int row, int k) {
  return (size_t)(k >> 5) * ((size_t)R * 32) + (size_t)row * 32 + ((((k >> 3) & 3) ^ swz(row)) << 3) + (k & 7);
}

template <int SG> __device__ __forceinline__ float2 cispi(float x) {
  float sn, cs; sincospif(x, &sn, &cs);
  return make_float2(cs, SG * sn);
}
template <int R> __device__ __forceinline__ void twiddle_pow(float2* v, float2 w1) {
  float2 w2 = cmul(w1, w1), w3 = cmul(w2, w1), w4 = cmul(w2, w2);
  v[1] = cmul(v[1], w1); v[2] = cmul(v[2], w2); v[3] = cmul(v[3], w3);
  if (R > 4) {
    float2 w5 = cmul(w4, w1), w6 = cmul(w3, w3), w7 = cmul(w4, w3);
    v[4] = cmul(v[4], w4); v[5] = cmul(v[5], w5); v[6] = cmul(v[6], w6); v[7] = cmul(v[7], w7);
    if (R > 8) { float2 w8 = cmul(w4, w4), w9 = cmul(w8, w1); v[8] = cmul(v[8], w8); v[9] = cmul(v[9], w9); }
  }
}

template <int SG>
__device__ __forceinline__ void fft640(float2* b0, float2* b1, int j, float2 w2_, float2 w3_, float2 w4_) {
  float2 v[10];
#pragma unroll
  for (int r = 0; r < 4; ++r) v[r] = b0[PD(j + 160 * r)];
  dft4<SG>(v[0], v[1], v[2], v[3]);
#pragma unroll
  for (int r = 0; r < 4; ++r) b1[PD(4 * j + r)] = v[r];
  __syncthreads();
#pragma unroll
  for (int r = 0; r < 4; ++r) v[r] = b1[PD(j + 160 * r)];
  twiddle_pow<4>(v, w2_);
  dft4<SG>(v[0], v[1], v[2], v[3]);
#pragma unroll
  for (int r = 0; r < 4; ++r) b0[PD((j >> 2) * 16 + (j & 3) + 4 * r)] = v[r];
  __syncthreads();
#pragma unroll
  for (int r = 0; r < 4; ++r) v[r] = b0[PD(j + 160 * r)];
  twiddle_pow<4>(v, w3_);
  dft4<SG>(v[0], v[1], v[2], v[3]);
#pragma unroll
  for (int r = 0; r < 4; ++r) b1[PD((j >> 4) * 64 + (j & 15) + 16 * r)] = v[r];
  __syncthreads();
  if (j < 64) {
#pragma unroll
    for (int r = 0; r < 10; ++r) v[r] = b1[PD(j + 64 * r)];
    twiddle_pow<10>(v, w4_);
    dft10<SG>(v);
#pragma unroll
    for (int r = 0; r < 10; ++r) b0[PD(j + 64 * r)] = v[r];
  }
}

__device__ __forceinline__ void ln8(float* xv, float* red, int row, int c) {
  float s = 0.f;
#pragma unroll
  for (int i = 0; i < 8; ++i) s += xv[i];
  s += __shfl_xor(s, 1); s += __shfl_xor(s, 2); s += __shfl_xor(s, 4);
  if ((c & 7) == 0) red[row * 10 + (c >> 3)] = s;
  __syncthreads();
  float mean = 0.f;
#pragma unroll
  for (int i = 0; i < 10; ++i) mean += red[row * 10 + i];
  mean *= (1.0f / 640.0f);
  float q = 0.f;
#pragma unroll
  for (int i = 0; i < 8; ++i) { float d = xv[i] - mean; q += d * d; }
  q += __shfl_xor(q, 1); q += __shfl_xor(q, 2); q += __shfl_xor(q, 4);
  if ((c & 7) == 0) red[80 + row * 10 + (c >> 3)] = q;
  __syncthreads();
  float var = 0.f;
#pragma unroll
  for (int i = 0; i < 10; ++i) var += red[80 + row * 10 + i];
  var *= (1.0f / 640.0f);
  float mb = rbf(mean), vb = rbf(var);
  float den = rbf(sqrtf(rbf(vb + 1e-5f)));
  float inv = 1.0f / den;
#pragma unroll
  for (int i = 0; i < 8; ++i) xv[i] = rbf(rbf(xv[i] - mb) * inv);
}

#define STAMP_DECL
#define STAMP_AT()
#define STAMP_OUT(w)
constexpr int FB = 720;

__global__ __launch_bounds__(640) void k_a1(const u16* __restrict__ x, float2* __restrict__ Zt,
                                            const u16* __restrict__ wr, const u16* __restrict__ wi,
                                            unsigned* __restrict__ Wt) {
  __shared__ float2 buf[2][4][FB];
  __shared__ float red[160];
  const int t = threadIdx.x;
  if (blockIdx.x >= 256) {
    const int s0 = (blockIdx.x - 256) * 16 + (t >= 320 ? 8 : 0);
    for (int k = t % 320; k < DF_; k += 320) {
      unsigned p[8];
#pragma unroll
      for (int r = 0; r < 8; ++r)
        p[r] = (unsigned)wr[(s0 + r) * DF_ + k] | ((unsigned)wi[(s0 + r) * DF_ + k] << 16);
      uint4* dst = (uint4*)(Wt + (size_t)k * S_ + s0);
      dst[0] = make_uint4(p[0], p[1], p[2], p[3]);
      dst[1] = make_uint4(p[4], p[5], p[6], p[7]);
    }
    return;
  }
  const int s0 = blockIdx.x * 8;
  STAMP_DECL STAMP_AT();
  const int row = t / 80, c = t % 80;
  float xv[8];
  {
    uint4 a = *(const uint4*)(x + (size_t)(s0 + row) * D_ + c * 8);
    unsigned u[4] = {a.x, a.y, a.z, a.w};
#pragma unroll
    for (int i = 0; i < 4; ++i) { xv[2 * i] = __uint_as_float(u[i] << 16); xv[2 * i + 1] = __uint_as_float(u[i] & 0xffff0000u); }
  }
  const int f = t / 160, j = t % 160;
  const float2 w2_ = cispi<-1>((float)(j & 3) * (1.0f / 8.0f)), w3_ = cispi<-1>((float)(j & 15) * (1.0f / 32.0f));
  const float2 w4_ = cispi<-1>((float)j * (1.0f / 320.0f));
  STAMP_AT();
  ln8(xv, red, row, c);
  STAMP_AT();
  {
    float* dst = (float*)&buf[0][row >> 1][0] + (row & 1);
#pragma unroll
    for (int i = 0; i < 8; ++i) dst[2 * PD(c * 8 + i)] = xv[i];
  }
  __syncthreads();
  fft640<-1>(buf[0][f], buf[1][f], j, w2_, w3_, w4_);
  __syncthreads();
  STAMP_AT();
#pragma unroll
  for (int i = 0; i < 3; ++i) {
    int e = t + 640 * i;
    if (i < 2 || e < DF_ * 4) {
      int k = e >> 2, ff = e & 3;
      float2 a = buf[0][ff][PD(k)], b = buf[0][ff][PD(k == 0 ? 0 : 640 - k)];
      float4 o;
      o.x = 0.5f * (a.x + b.x); o.y = 0.5f * (a.y - b.y);
      o.z = 0.5f * (a.y + b.y); o.w = 0.5f * (b.x - a.x);
      *(float4*)(Zt + (size_t)k * S_ + s0 + 2 * ff) = o;
    }
  }
  STAMP_AT();
  STAMP_OUT(2);
}

__device__ __forceinline__ void pack_w13(const u16* __restrict__ src, u16* __restrict__ dst, int which, int nb, int k0, u16* sm) {
  const int t = threadIdx.x;
  for (int e = t; e < 64 * 27; e += 256) {
    int r = e / 27, q = e % 27;
    uint2 v = *(const uint2*)(src + (size_t)(k0 + r) * F_ + nb * 108 + q * 4);
    sm[(q * 4 + 0) * 66 + r] = (u16)(v.x & 0xffff); sm[(q * 4 + 1) * 66 + r] = (u16)(v.x >> 16);
    sm[(q * 4 + 2) * 66 + r] = (u16)(v.y & 0xffff); sm[(q * 4 + 3) * 66 + r] = (u16)(v.y >> 16);
  }
  __syncthreads();
  for (int e = t; e < 112 * 8; e += 256) {
    int c = e >> 3, q = e & 7;
    uint4 v = make_uint4(0, 0, 0, 0);
    if (c < 108) {
      u16* pp = (u16*)&v;
#pragma unroll
      for (int i = 0; i < 8; ++i) pp[i] = sm[c * 66 + q * 8 + i];
    }
    int drow = nb * 224 + (c >> 4) * 32 + which * 16 + (c & 15);
    *(uint4*)(dst + kb_off(16 * 224, drow, k0 + q * 8)) = v;
  }
}
__device__ __forceinline__ void pack_w2(const u16* __restrict__ src, u16* __restrict__ dst, int nb, int d0, u16* sm) {
  const int t = threadIdx.x;
  for (int e = t; e < 108 * 8; e += 256) {
    int i = e >> 3, q = e & 7;
    uint4 v = *(const uint4*)(src + (size_t)(nb * 108 + i) * D_ + d0 + q * 8);
    u16* pp = (u16*)&v;
#pragma unroll
    for (int j = 0; j < 8; ++j) sm[(q * 8 + j) * 114 + i] = pp[j];
  }
  __syncthreads();
  for (int e = t; e < 64 * 14; e += 256) {
    int d = e / 14, q = e % 14;
    uint4 v; u16* pp = (u16*)&v;
#pragma unroll
    for (int j = 0; j < 8; ++j) { int i = q * 8 + j; pp[j] = i < 108 ? sm[d * 114 + i] : (u16)0; }
    *(uint4*)(dst + kb_off(D_, d0 + d, nb * 112 + q * 8)) = v;
  }
}

constexpr int CB = 2048 + 256;
__global__ __launch_bounds__(256) void k_a2(float2* __restrict__ Zt, const unsigned* __restrict__ Wt,
                                            const u16* __restrict__ w1, const u16* __restrict__ w3, const u16* __restrict__ w2,
                                            u16* __restrict__ w13p, u16* __restrict__ w2t) {
  __shared__ float2 smem[2 * CB];
  const int t = threadIdx.x;
  if (blockIdx.x >= DF_) {
    int id = blockIdx.x - DF_;
    u16* sm = (u16*)smem;
    if (id < 320) {
      int which = id / 160, r = id % 160;
      pack_w13(which ? w3 : w1, w13p, which, r / 10, (r % 10) * 64, sm);
    } else {
      int r = id - 320;
      pack_w2(w2, w2t, r / 10, (r % 10) * 64, sm);
    }
    return;
  }
  float2* b0 = smem; float2* b1 = smem + CB;
  const int k = blockIdx.x;
  float2* col = Zt + (size_t)k * S_;
  STAMP_DECL STAMP_AT();
  float2 v[8];
#pragma unroll
  for (int r = 0; r < 8; ++r) v[r] = col[t + 256 * r];
  unsigned wv[8];
#pragma unroll
  for (int r = 0; r < 8; ++r) wv[r] = Wt[(size_t)k * S_ + t + 256 * r];
  const float2 wp2 = cispi<-1>((float)(t & 7) * (1.0f / 32.0f));
  const float2 wp3 = cispi<-1>((float)(t & 63) * (1.0f / 256.0f));
  const float2 wp4a = cispi<-1>((float)t * (1.0f / 1024.0f)), wp4b = cispi<-1>((float)(t + 256) * (1.0f / 1024.0f));
  dft8<-1>(v);
  STAMP_AT();
#pragma unroll
  for (int r = 0; r < 8; ++r) b0[PD(8 * t + r)] = v[r];
  __syncthreads();
  {
    int m = t & 7;
#pragma unroll
    for (int r = 0; r < 8; ++r) v[r] = b0[PD(t + 256 * r)];
    twiddle_pow<8>(v, wp2);
    dft8<-1>(v);
#pragma unroll
    for (int r = 0; r < 8; ++r) b1[PD((t >> 3) * 64 + m + 8 * r)] = v[r];
  }
  __syncthreads();
  {
    int m = t & 63;
#pragma unroll
    for (int r = 0; r < 8; ++r) v[r] = b1[PD(t + 256 * r)];
    twiddle_pow<8>(v, wp3);
    dft8<-1>(v);
#pragma unroll
    for (int r = 0; r < 8; ++r) b0[PD((t >> 6) * 512 + m + 64 * r)] = v[r];
  }
  __syncthreads();
#pragma unroll
  for (int h = 0; h < 2; ++h) {
    int j = t + 256 * h;
    float2 a[4] = {b0[PD(j)], b0[PD(j + 512)], b0[PD(j + 1024)], b0[PD(j + 1536)]};
    twiddle_pow<4>(a, h ? wp4b : wp4a);
    dft4<-1>(a[0], a[1], a[2], a[3]);
    v[h] = a[0]; v[h + 2] = a[1]; v[h + 4] = a[2]; v[h + 6] = a[3];
  }
  STAMP_AT();
  const float sc = 1.0f / (2048.0f * 640.0f);
#pragma unroll
  for (int r = 0; r < 8; ++r) {
    float2 w = make_float2(__uint_as_float(wv[r] << 16) * sc, __uint_as_float(wv[r] & 0xffff0000u) * sc);
    v[r] = cmul(v[r], w);
  }
  dft8<1>(v);
#pragma unroll
  for (int r = 0; r < 8; ++r) b1[PD(8 * t + r)] = v[r];
  __syncthreads();
  {
    int m = t & 7;
#pragma unroll
    for (int r = 0; r < 8; ++r) v[r] = b1[PD(t + 256 * r)];
    twiddle_pow<8>(v, twc<1>(wp2));
    dft8<1>(v);
#pragma unroll
    for (int r = 0; r < 8; ++r) b0[PD((t >> 3) * 64 + m + 8 * r)] = v[r];
  }
  __syncthreads();
  {
    int m = t & 63;
#pragma unroll
    for (int r = 0; r < 8; ++r) v[r] = b0[PD(t + 256 * r)];
    twiddle_pow<8>(v, twc<1>(wp3));
    dft8<1>(v);
#pragma unroll
    for (int r = 0; r < 8; ++r) b1[PD((t >> 6) * 512 + m + 64 * r)] = v[r];
  }
  __syncthreads();
#pragma unroll
  for (int h = 0; h < 2; ++h) {
    int j = t + 256 * h;
    float2 a[4] = {b1[PD(j)], b1[PD(j + 512)], b1[PD(j + 1024)], b1[PD(j + 1536)]};
    twiddle_pow<4>(a, twc<1>(h ? wp4b : wp4a));
    dft4<1>(a[0], a[1], a[2], a[3]);
    col[j] = a[0]; col[j + 512] = a[1]; col[j + 1024] = a[2]; col[j + 1536] = a[3];
  }
  STAMP_AT();
  STAMP_OUT(3);
}

__global__ __launch_bounds__(640) void k_a3(const float2* __restrict__ Zt, const u16* xin,
                                            u16* xout, u16* __restrict__ xln) {
  __shared__ float2 buf[2][4][FB];
  __shared__ float red[160];
  const int t = threadIdx.x, s0 = blockIdx.x * 8;
  STAMP_DECL STAMP_AT();
  float4 z[3];
#pragma unroll
  for (int i = 0; i < 3; ++i) {
    int e = t + 640 * i;
    if (i < 2 || e < DF_ * 4) z[i] = *(const float4*)(Zt + (size_t)(e >> 2) * S_ + s0 + 2 * (e & 3));
  }
  const int row = t / 80, c = t % 80;
  float xv[8];
  {
    uint4 a = *(const uint4*)(xin + (size_t)(s0 + row) * D_ + c * 8);
    unsigned u[4] = {a.x, a.y, a.z, a.w};
#pragma unroll
    for (int i = 0; i < 4; ++i) { xv[2 * i] = __uint_as_float(u[i] << 16); xv[2 * i + 1] = __uint_as_float(u[i] & 0xffff0000u); }
  }
  const int f = t / 160, j = t % 160;
  const float2 w2_ = cispi<1>((float)(j & 3) * (1.0f / 8.0f)), w3_ = cispi<1>((float)(j & 15) * (1.0f / 32.0f));
  const float2 w4_ = cispi<1>((float)j * (1.0f / 320.0f));
#pragma unroll
  for (int i = 0; i < 3; ++i) {
    int e = t + 640 * i;
    if (i < 2 || e < DF_ * 4) {
      int k = e >> 2, ff = e & 3;
      if (k == 0 || k == 320) {
        buf[0][ff][PD(k)] = make_float2(z[i].x, z[i].z);
      } else {
        buf[0][ff][PD(k)] = make_float2(z[i].x - z[i].w, z[i].y + z[i].z);
        buf[0][ff][PD(640 - k)] = make_float2(z[i].x + z[i].w, z[i].z - z[i].y);
      }
    }
  }
  __syncthreads();
  STAMP_AT();
  fft640<1>(buf[0][f], buf[1][f], j, w2_, w3_, w4_);
  __syncthreads();
  STAMP_AT();
  {
    const float* src = (const float*)&buf[0][row >> 1][0] + (row & 1);
    unsigned u[4];
#pragma unroll
    for (int i = 0; i < 8; ++i) xv[i] = rbf(xv[i] + rbf(src[2 * PD(c * 8 + i)]));
#pragma unroll
    for (int i = 0; i < 4; ++i) u[i] = (__float_as_uint(xv[2 * i]) >> 16) | (__float_as_uint(xv[2 * i + 1]) & 0xffff0000u);
    *(uint4*)(xout + (size_t)(s0 + row) * D_ + c * 8) = make_uint4(u[0], u[1], u[2], u[3]);
  }
  ln8(xv, red, row, c);
  {
    unsigned u[4];
#pragma unroll
    for (int i = 0; i < 4; ++i) u[i] = (__float_as_uint(xv[2 * i]) >> 16) | (__float_as_uint(xv[2 * i + 1]) & 0xffff0000u);
    *(uint4*)(xln + kb_off(S_, s0 + row, c * 8)) = make_uint4(u[0], u[1], u[2], u[3]);
  }
  STAMP_AT();
  STAMP_OUT(4);
}

template <int N> __device__ __forceinline__ void wait_vm() { asm volatile("s_waitcnt vmcnt(%0)" ::"i"(N) : "memory"); }
__device__ __forceinline__ void wait_lgkm0() { asm volatile("s_waitcnt lgkmcnt(0)" ::: "memory"); }

constexpr int FP_ = 1792;

template <int NP, int L>
__device__ __forceinline__ void g1_body(const u16* __restrict__ A, const u16* __restrict__ Bt, u16* __restrict__ out,
                                        char* lds, int bm, int bn, int wm, int p0) {
  constexpr int BM = 128, BN = 224, NS = 6, NT = 20, PF = 5;
  constexpr int ABYTES = BM * 64, BBYTES = BN * 64, STG = ABYTES + BBYTES;
  const int t = threadIdx.x, lane = t & 63, fr = lane & 15, fq = lane >> 4;
  const char* Ag = (const char*)A + (size_t)bm * ABYTES + t * 16;
  const char* Bg = (const char*)Bt + (size_t)bn * BBYTES + t * 16;
  auto stage = [&](int ks) {
    char* la = lds + (ks % NS) * STG + t * 16;
    __builtin_amdgcn_global_load_lds((const unsigned*)(Ag + (size_t)ks * (S_ * 64)), (unsigned*)la, 16, 0, 0);
    __builtin_amdgcn_global_load_lds((const unsigned*)(Bg + (size_t)ks * (3584 * 64)), (unsigned*)(la + ABYTES), 16, 0, 0);
    if (L == 3)
      __builtin_amdgcn_global_load_lds((const unsigned*)(Bg + (size_t)ks * (3584 * 64) + 8192), (unsigned*)(la + ABYTES + 8192), 16, 0, 0);
  };
  const int aoff = (wm * 32 + fr) * 64 + ((fq ^ swz(fr)) << 4);
  const int boff = ABYTES + (p0 * 32 + fr) * 64 + ((fq ^ swz(fr)) << 4);
  bf16x8 af[2][2], bfr[2][2 * NP];
  auto ldfr = [&](int set, int ks) {
    const char* l = lds + (ks % NS) * STG;
#pragma unroll
    for (int m = 0; m < 2; ++m) af[set][m] = *(const bf16x8*)(l + aoff + m * 1024);
#pragma unroll
    for (int n = 0; n < 2 * NP; ++n) bfr[set][n] = *(const bf16x8*)(l + boff + n * 1024);
  };
  f32x4 acc[2][2 * NP];
#pragma unroll
  for (int m = 0; m < 2; ++m)
#pragma unroll
    for (int n = 0; n < 2 * NP; ++n) acc[m][n] = (f32x4){0.f, 0.f, 0.f, 0.f};
  STAMP_DECL STAMP_AT();
#pragma unroll
  for (int i = 0; i < PF; ++i) stage(i);
  wait_vm<(PF - 1) * L>();
  __builtin_amdgcn_s_barrier();
  ldfr(0, 0);
#pragma unroll
  for (int ks = 0; ks < NT; ++ks) {
    STAMP_AT();
    const int cur = ks & 1;
    if (ks + 1 < NT) {
      constexpr int dummy = 0; (void)dummy;
      const int issued = (ks + PF - 1 < NT - 1) ? ks + PF - 1 : NT - 1;
      const int allow = (issued - (ks + 1)) * L;
      if (allow >= 3 * L) wait_vm<3 * L>(); else if (allow == 2 * L) wait_vm<2 * L>(); else if (allow == L) wait_vm<L>(); else wait_vm<0>();
      __builtin_amdgcn_s_barrier();
      if (ks + PF < NT) stage(ks + PF);
      ldfr(cur ^ 1, ks + 1);
    }
#pragma unroll
    for (int n = 0; n < 2 * NP; ++n)
#pragma unroll
      for (int m = 0; m < 2; ++m)
        acc[m][n] = __builtin_amdgcn_mfma_f32_16x16x32_bf16(bfr[cur][n], af[cur][m], acc[m][n], 0, 0, 0);
    if (ks + 1 < NT) {
#pragma unroll
      for (int g = 0; g < L; ++g) {
        __builtin_amdgcn_sched_group_barrier(0x8, 1, 0);
        __builtin_amdgcn_sched_group_barrier(0x10, 1, 0);
      }
#pragma unroll
      for (int g = 0; g < 2 * NP + 2; ++g) {
        __builtin_amdgcn_sched_group_barrier(0x8, 1, 0);
        __builtin_amdgcn_sched_group_barrier(0x100, 1, 0);
      }
    }
    __builtin_amdgcn_sched_barrier(0);
  }
  STAMP_AT();
#pragma unroll
  for (int m = 0; m < 2; ++m) {
    int row = bm * BM + wm * 32 + m * 16 + fr;
#pragma unroll
    for (int p = 0; p < NP; ++p) {
      int hc = bn * 112 + (p0 + p) * 16 + fq * 4;
      u16 o[4];
#pragma unroll
      for (int j = 0; j < 4; ++j) {
        float g = rbf(acc[m][2 * p][j]), u = rbf(acc[m][2 * p + 1][j]);
        float sg = rbf(g * __builtin_amdgcn_rcpf(1.0f + __expf(-g)));
        o[j] = f2bf(sg * u);
      }
      *(uint2*)(out + kb_off(S_, row, hc)) = make_uint2(o[0] | ((unsigned)o[1] << 16), o[2] | ((unsigned)o[3] << 16));
    }
  }
  STAMP_AT();
  STAMP_OUT(0);
}
__global__ __launch_bounds__(512) void k_g1(const u16* __restrict__ A, const u16* __restrict__ Bt, u16* __restrict__ out) {
  __shared__ __attribute__((aligned(16))) char lds[6 * (128 + 224) * 64];
  const int wid = __builtin_amdgcn_readfirstlane(threadIdx.x >> 6);
  const int xcd = blockIdx.x & 7, idx = blockIdx.x >> 3;
  const int bm = (xcd >> 1) * 4 + (idx & 3), bn = (xcd & 1) * 8 + (idx >> 2);
  if (wid < 4) g1_body<4, 3>(A, Bt, out, lds, bm, bn, wid, 0);
  else if (wid < 6) g1_body<3, 3>(A, Bt, out, lds, bm, bn, wid - 4, 4);
  else g1_body<3, 2>(A, Bt, out, lds, bm, bn, wid - 4, 4);
}

template <int L>
__device__ __forceinline__ void g2_body(const u16* __restrict__ A, const u16* __restrict__ Bt, const u16* res, u16* out,
                                        char* lds, int bm, int bn, int wid) {
  constexpr int BM = 64, BN = 80, NS = 4, NT = 14, PF = 3;
  constexpr int ABYTES = 4 * BM * 64, BBYTES = 4 * BN * 64, STG = ABYTES + BBYTES;
  const int t = threadIdx.x, lane = t & 63, fr = lane & 15, fq = lane >> 4;
  const int ks4 = wid & 3, mh = wid >> 2;
  const char* Ag[2]; const char* Bg[3];
#pragma unroll
  for (int i = 0; i < 2; ++i) { int by = t * 16 + i * 8192; Ag[i] = (const char*)A + (size_t)(by >> 12) * (S_ * 64) + (size_t)bm * 4096 + (by & 4095); }
#pragma unroll
  for (int i = 0; i < 3; ++i) { int by = t * 16 + i * 8192; int kb = by / 5120; Bg[i] = (const char*)Bt + (size_t)kb * (D_ * 64) + (size_t)bn * 5120 + (by - kb * 5120); }
  auto stage = [&](int st) {
    char* la = lds + (st % NS) * STG + t * 16;
#pragma unroll
    for (int i = 0; i < 2; ++i)
      __builtin_amdgcn_global_load_lds((const unsigned*)(Ag[i] + (size_t)st * (4 * S_ * 64)), (unsigned*)(la + i * 8192), 16, 0, 0);
#pragma unroll
    for (int i = 0; i < L - 2; ++i)
      __builtin_amdgcn_global_load_lds((const unsigned*)(Bg[i] + (size_t)st * (4 * D_ * 64)), (unsigned*)(la + ABYTES + i * 8192), 16, 0, 0);
  };
  const int aoff = ks4 * 4096 + (mh * 32 + fr) * 64 + ((fq ^ swz(fr)) << 4);
  const int boff = ABYTES + ks4 * 5120 + fr * 64 + ((fq ^ swz(fr)) << 4);
  bf16x8 af[2][2], bfr[2][5];
  auto ldfr = [&](int set, int st) {
    const char* l = lds + (st % NS) * STG;
#pragma unroll
    for (int m = 0; m < 2; ++m) af[set][m] = *(const bf16x8*)(l + aoff + m * 1024);
#pragma unroll
    for (int n = 0; n < 5; ++n) bfr[set][n] = *(const bf16x8*)(l + boff + n * 1024);
  };
  f32x4 acc[2][5];
#pragma unroll
  for (int m = 0; m < 2; ++m)
#pragma unroll
    for (int n = 0; n < 5; ++n) acc[m][n] = (f32x4){0.f, 0.f, 0.f, 0.f};
  STAMP_DECL STAMP_AT();
  uint2 rvp[3];
#pragma unroll
  for (int i = 0; i < 3; ++i) {
    int fi = wid + 8 * i; if (fi >= 20) fi = wid;
    rvp[i] = *(const uint2*)(res + (size_t)(bm * BM + (fi / 5) * 16 + fr) * D_ + bn * BN + (fi % 5) * 16 + fq * 4);
  }
#pragma unroll
  for (int i = 0; i < PF; ++i) stage(i);
  wait_vm<(PF - 1) * L>();
  __builtin_amdgcn_s_barrier();
  ldfr(0, 0);
#pragma unroll
  for (int st = 0; st < NT; ++st) {
    STAMP_AT();
    const int cur = st & 1;
    if (st + 1 < NT) {
      const int issued = (st + PF - 1 < NT - 1) ? st + PF - 1 : NT - 1;
      const int allow = (issued - (st + 1)) * L;
      if (allow >= L) wait_vm<L>(); else wait_vm<0>();
      __builtin_amdgcn_s_barrier();
      if (st + PF < NT) stage(st + PF);
      ldfr(cur ^ 1, st + 1);
    }
#pragma unroll
    for (int m = 0; m < 2; ++m)
#pragma unroll
      for (int n = 0; n < 5; ++n)
        acc[m][n] = __builtin_amdgcn_mfma_f32_16x16x32_bf16(bfr[cur][n], af[cur][m], acc[m][n], 0, 0, 0);
    if (st + 1 < NT) {
#pragma unroll
      for (int g = 0; g < 5; ++g) {
        __builtin_amdgcn_sched_group_barrier(0x8, 1, 0);
        __builtin_amdgcn_sched_group_barrier(0x10, 1, 0);
        __builtin_amdgcn_sched_group_barrier(0x100, 1, 0);
      }
#pragma unroll
      for (int g = 0; g < 2; ++g) {
        __builtin_amdgcn_sched_group_barrier(0x8, 1, 0);
        __builtin_amdgcn_sched_group_barrier(0x100, 1, 0);
      }
    }
    __builtin_amdgcn_sched_barrier(0);
  }
  STAMP_AT();
  __syncthreads();
  f32x4* red = (f32x4*)lds;
#pragma unroll
  for (int m = 0; m < 2; ++m)
#pragma unroll
    for (int n = 0; n < 5; ++n) red[(ks4 * 20 + mh * 10 + m * 5 + n) * 64 + lane] = acc[m][n];
  __syncthreads();
#pragma unroll
  for (int i = 0; i < 3; ++i) {
    int fi = wid + 8 * i;
    if (fi < 20) {
      f32x4 s = red[fi * 64 + lane] + red[(20 + fi) * 64 + lane] + red[(40 + fi) * 64 + lane] + red[(60 + fi) * 64 + lane];
      int mrow = fi / 5, n = fi % 5;
      int row = bm * BM + mrow * 16 + fr;
      int cc = bn * BN + n * 16 + fq * 4;
      uint2 rv = rvp[i];
      float r0 = __uint_as_float(rv.x << 16), r1 = __uint_as_float(rv.x & 0xffff0000u);
      float r2 = __uint_as_float(rv.y << 16), r3 = __uint_as_float(rv.y & 0xffff0000u);
      u16 o0 = f2bf(r0 + rbf(s[0])), o1 = f2bf(r1 + rbf(s[1]));
      u16 o2 = f2bf(r2 + rbf(s[2])), o3 = f2bf(r3 + rbf(s[3]));
      *(uint2*)(out + (size_t)row * D_ + cc) = make_uint2(o0 | ((unsigned)o1 << 16), o2 | ((unsigned)o3 << 16));
    }
  }
  STAMP_AT();
  STAMP_OUT(1);
}
__global__ __launch_bounds__(512) void k_g2(const u16* __restrict__ A, const u16* __restrict__ Bt, const u16* res, u16* out) {
  __shared__ __attribute__((aligned(16))) char lds[4 * 4 * (64 + 80) * 64];
  const int wid = __builtin_amdgcn_readfirstlane(threadIdx.x >> 6);
  const int xcd = blockIdx.x & 7, idx = blockIdx.x >> 3;
  const int bm = xcd * 4 + (idx & 3), bn = idx >> 2;
  if (wid < 4) g2_body<5>(A, Bt, res, out, lds, bm, bn, wid);
  else g2_body<4>(A, Bt, res, out, lds, bm, bn, wid);
}

extern "C" void kernel_launch(void* const* d_in, const int* in_sizes, int n_in,
                              void* d_out, int out_size, void* d_ws, size_t ws_size,
                              hipStream_t stream) {
  const u16* Ax = (const u16*)d_in[0];
  const u16* wr = (const u16*)d_in[1];
  const u16* wi = (const u16*)d_in[2];
  const u16* w1 = (const u16*)d_in[4];
  const u16* w3 = (const u16*)d_in[5];
  const u16* w2 = (const u16*)d_in[6];
  char* ws = (char*)d_ws;
  size_t off = 0;
  auto alloc = [&](size_t bytes) { void* p = ws + off; off += (bytes + 255) & ~(size_t)255; return p; };
  float2* Zt = (float2*)alloc((size_t)DF_ * S_ * 8);
  unsigned* Wt = (unsigned*)alloc((size_t)DF_ * S_ * 4);
  u16* xb = (u16*)alloc((size_t)S_ * D_ * 2);
  u16* xln = (u16*)alloc((size_t)S_ * D_ * 2);
  u16* h = (u16*)alloc((size_t)S_ * 1792 * 2);
  u16* w13p = (u16*)alloc((size_t)16 * 224 * D_ * 2);
  u16* w2t = (u16*)alloc((size_t)D_ * 1792 * 2);
  u16* outp = (u16*)d_out;

  for (int layer = 0; layer < 2; ++layer) {
    const u16* xin = layer == 0 ? Ax : xb;
    k_a1<<<layer == 0 ? 256 + 128 : 256, 640, 0, stream>>>(xin, Zt, wr, wi, Wt);
    k_a2<<<layer == 0 ? DF_ + 480 : DF_, 256, 0, stream>>>(Zt, Wt, w1, w3, w2, w13p, w2t);
    k_a3<<<256, 640, 0, stream>>>(Zt, xin, xb, xln);
    k_g1<<<256, 512, 0, stream>>>(xln, w13p, h);
    k_g2<<<256, 512, 0, stream>>>(h, w2t, xb, layer == 0 ? xb : outp);
  }
}
```

```cpp
#include <hip/hip_runtime.h>
#include <hip/hip_bf16.h>
#include <stdint.h>

typedef unsigned short u16;
using bf16x8 = __attribute__((ext_vector_type(8))) short;
using f32x4 = __attribute__((ext_vector_type(4))) float;

constexpr int S_ = 2048, D_ = 640, DF_ = 321, F_ = 1728;

__device__ __forceinline__ float bf2f(u16 v) { return __uint_as_float(((unsigned)v) << 16); }
__device__ __forceinline__ u16 f2bf(float f) { return __bfloat16_as_ushort(__float2bfloat16(f)); }
__device__ __forceinline__ float rbf(float f) { return bf2f(f2bf(f)); }

__device__ __forceinline__ float2 cadd(float2 a, float2 b) { return make_float2(a.x + b.x, a.y + b.y); }
__device__ __forceinline__ float2 csub(float2 a, float2 b) { return make_float2(a.x - b.x, a.y - b.y); }
__device__ __forceinline__ float2 cmul(float2 a, float2 b) {
  return make_float2(a.x * b.x - a.y * b.y, a.x * b.y + a.y * b.x);
}
template <int SG> __device__ __forceinline__ float2 mul_i(float2 a) {
  return SG > 0 ? make_float2(-a.y, a.x) : make_float2(a.y, -a.x);
}
template <int SG> __device__ __forceinline__ float2 twc(float2 w) {
  return SG > 0 ? make_float2(w.x, -w.y) : w;
}

template <int SG> __device__ __forceinline__ void dft4(float2& a0, float2& a1, float2& a2, float2& a3) {
  float2 s0 = cadd(a0, a2), d0 = csub(a0, a2), s1 = cadd(a1, a3), d1 = mul_i<SG>(csub(a1, a3));
  a0 = cadd(s0, s1); a2 = csub(s0, s1); a1 = cadd(d0, d1); a3 = csub(d0, d1);
}
template <int SG> __device__ __forceinline__ void dft8(float2* v) {
  float2 e0 = v[0], e1 = v[2], e2 = v[4], e3 = v[6], o0 = v[1], o1 = v[3], o2 = v[5], o3 = v[7];
  dft4<SG>(e0, e1, e2, e3); dft4<SG>(o0, o1, o2, o3);
  const float h = 0.70710678118654752440f;
  float2 t1 = make_float2(h * (o1.x - SG * o1.y), h * (o1.y + SG * o1.x));
  float2 t2 = mul_i<SG>(o2);
  float2 t3 = make_float2(h * (-o3.x - SG * o3.y), h * (-o3.y + SG * o3.x));
  v[0] = cadd(e0, o0); v[4] = csub(e0, o0);
  v[1] = cadd(e1, t1); v[5] = csub(e1, t1);
  v[2] = cadd(e2, t2); v[6] = csub(e2, t2);
  v[3] = cadd(e3, t3); v[7] = csub(e3, t3);
}
template <int SG> __device__ __forceinline__ void dft5(float2& a0, float2& a1, float2& a2, float2& a3, float2& a4) {
  const float c1 = 0.30901699437494742410f, c2 = -0.80901699437494742410f;
  const float s1 = 0.95105651629515357212f, s2 = 0.58778525229247312917f;
  float2 p1 = cadd(a1, a4), m1 = csub(a1, a4), p2 = cadd(a2, a3), m2 = csub(a2, a3);
  float2 t1 = make_float2(a0.x + c1 * p1.x + c2 * p2.x, a0.y + c1 * p1.y + c2 * p2.y);
  float2 t2 = make_float2(a0.x + c2 * p1.x + c1 * p2.x, a0.y + c2 * p1.y + c1 * p2.y);
  float2 u1 = mul_i<SG>(make_float2(s1 * m1.x + s2 * m2.x, s1 * m1.y + s2 * m2.y));
  float2 u2 = mul_i<SG>(make_float2(s2 * m1.x - s1 * m2.x, s2 * m1.y - s1 * m2.y));
  a0 = cadd(a0, cadd(p1, p2));
  a1 = cadd(t1, u1); a4 = csub(t1, u1);
  a2 = cadd(t2, u2); a3 = csub(t2, u2);
}
template <int SG> __device__ __forceinline__ void dft10(float2* v) {
  float2 e0 = v[0], e1 = v[2], e2 = v[4], e3 = v[6], e4 = v[8];
  float2 o0 = v[1], o1 = v[3], o2 = v[5], o3 = v[7], o4 = v[9];
  dft5<SG>(e0, e1, e2, e3, e4); dft5<SG>(o0, o1, o2, o3, o4);
  const float c1 = 0.80901699437494742410f, s1 = 0.58778525229247312917f;
  const float c2 = 0.30901699437494742410f, s2 = 0.95105651629515357212f;
  o1 = cmul(o1, make_float2(c1, SG * s1));
  o2 = cmul(o2, make_float2(c2, SG * s2));
  o3 = cmul(o3, make_float2(-c2, SG * s2));
  o4 = cmul(o4, make_float2(-c1, SG * s1));
  v[0] = cadd(e0, o0); v[5] = csub(e0, o0);
  v[1] = cadd(e1, o1); v[6] = csub(e1, o1);
  v[2] = cadd(e2, o2); v[7] = csub(e2, o2);
  v[3] = cadd(e3, o3); v[8] = csub(e3, o3);
  v[4] = cadd(e4, o4); v[9] = csub(e4, o4);
}

__device__ __forceinline__ int PD(int i) { return i + (i >> 3); }
__device__ __forceinline__ int swz(int row) { return (-(row >> 2)) & 3; }
__device__ __forceinline__ size_t kb_off(int R, int row, int k) {
  return (size_t)(k >> 5) * ((size_t)R * 32) + (size_t)row * 32 + ((((k >> 3) & 3) ^ swz(row)) << 3) + (k & 7);
}

template <int SG> __device__ __forceinline__ float2 cispi(float x) {
  float sn, cs; sincospif(x, &sn, &cs);
  return make_float2(cs, SG * sn);
}
template <int R> __device__ __forceinline__ void twiddle_pow(float2* v, float2 w1) {
  float2 w2 = cmul(w1, w1), w3 = cmul(w2, w1), w4 = cmul(w2, w2);
  v[1] = cmul(v[1], w1); v[2] = cmul(v[2], w2); v[3] = cmul(v[3], w3);
  if (R > 4) {
    float2 w5 = cmul(w4, w1), w6 = cmul(w3, w3), w7 = cmul(w4, w3);
    v[4] = cmul(v[4], w4); v[5] = cmul(v[5], w5); v[6] = cmul(v[6], w6); v[7] = cmul(v[7], w7);
    if (R > 8) { float2 w8 = cmul(w4, w4), w9 = cmul(w8, w1); v[8] = cmul(v[8], w8); v[9] = cmul(v[9], w9); }
  }
}

template <int SG>
__device__ __forceinline__ void fft640(float2* b0, float2* b1, int j, float2 w2_, float2 w3_, float2 w4_) {
  float2 v[10];
#pragma unroll
  for (int r = 0; r < 4; ++r) v[r] = b0[PD(j + 160 * r)];
  dft4<SG>(v[0], v[1], v[2], v[3]);
#pragma unroll
  for (int r = 0; r < 4; ++r) b1[PD(4 * j + r)] = v[r];
  __syncthreads();
#pragma unroll
  for (int r = 0; r < 4; ++r) v[r] = b1[PD(j + 160 * r)];
  twiddle_pow<4>(v, w2_);
  dft4<SG>(v[0], v[1], v[2], v[3]);
#pragma unroll
  for (int r = 0; r < 4; ++r) b0[PD((j >> 2) * 16 + (j & 3) + 4 * r)] = v[r];
  __syncthreads();
#pragma unroll
  for (int r = 0; r < 4; ++r) v[r] = b0[PD(j + 160 * r)];
  twiddle_pow<4>(v, w3_);
  dft4<SG>(v[0], v[1], v[2], v[3]);
#pragma unroll
  for (int r = 0; r < 4; ++r) b1[PD((j >> 4) * 64 + (j & 15) + 16 * r)] = v[r];
  __syncthreads();
  if (j < 64) {
#pragma unroll
    for (int r = 0; r < 10; ++r) v[r] = b1[PD(j + 64 * r)];
    twiddle_pow<10>(v, w4_);
    dft10<SG>(v);
#pragma unroll
    for (int r = 0; r < 10; ++r) b0[PD(j + 64 * r)] = v[r];
  }
}

__device__ __forceinline__ void ln8(float* xv, float* red, int row, int c) {
  float s = 0.f;
#pragma unroll
  for (int i = 0; i < 8; ++i) s += xv[i];
  s += __shfl_xor(s, 1); s += __shfl_xor(s, 2); s += __shfl_xor(s, 4);
  if ((c & 7) == 0) red[row * 10 + (c >> 3)] = s;
  __syncthreads();
  float mean = 0.f;
#pragma unroll
  for (int i = 0; i < 10; ++i) mean += red[row * 10 + i];
  mean *= (1.0f / 640.0f);
  float q = 0.f;
#pragma unroll
  for (int i = 0; i < 8; ++i) { float d = xv[i] - mean; q += d * d; }
  q += __shfl_xor(q, 1); q += __shfl_xor(q, 2); q += __shfl_xor(q, 4);
  if ((c & 7) == 0) red[80 + row * 10 + (c >> 3)] = q;
  __syncthreads();
  float var = 0.f;
#pragma unroll
  for (int i = 0; i < 10; ++i) var += red[80 + row * 10 + i];
  var *= (1.0f / 640.0f);
  float mb = rbf(mean), vb = rbf(var);
  float den = rbf(sqrtf(rbf(vb + 1e-5f)));
  float inv = 1.0f / den;
#pragma unroll
  for (int i = 0; i < 8; ++i) xv[i] = rbf(rbf(xv[i] - mb) * inv);
}

#define STAMP_DECL
#define STAMP_AT()
#define STAMP_OUT(w)
constexpr int FB = 720;

__device__ __forceinline__ void pack_w13(const u16* __restrict__ src, u16* __restrict__ dst, int which, int nb, int k0, u16* sm) {
  const int t = threadIdx.x;
  for (int e = t; e < 64 * 27; e += blockDim.x) {
    int r = e / 27, q = e % 27;
    uint2 v = *(const uint2*)(src + (size_t)(k0 + r) * F_ + nb * 108 + q * 4);
    sm[(q * 4 + 0) * 66 + r] = (u16)(v.x & 0xffff); sm[(q * 4 + 1) * 66 + r] = (u16)(v.x >> 16);
    sm[(q * 4 + 2) * 66 + r] = (u16)(v.y & 0xffff); sm[(q * 4 + 3) * 66 + r] = (u16)(v.y >> 16);
  }
  __syncthreads();
  for (int e = t; e < 112 * 8; e += blockDim.x) {
    int c = e >> 3, q = e & 7;
    uint4 v = make_uint4(0, 0, 0, 0);
    if (c < 108) {
      u16* pp = (u16*)&v;
#pragma unroll
      for (int i = 0; i < 8; ++i) pp[i] = sm[c * 66 + q * 8 + i];
    }
    int drow = nb * 224 + (c >> 4) * 32 + which * 16 + (c & 15);
    *(uint4*)(dst + kb_off(16 * 224, drow, k0 + q * 8)) = v;
  }
}
__device__ __forceinline__ void pack_w2(const u16* __restrict__ src, u16* __restrict__ dst, int nb, int d0, u16* sm) {
  const int t = threadIdx.x;
  for (int e = t; e < 108 * 8; e += blockDim.x) {
    int i = e >> 3, q = e & 7;
    uint4 v = *(const uint4*)(src + (size_t)(nb * 108 + i) * D_ + d0 + q * 8);
    u16* pp = (u16*)&v;
#pragma unroll
    for (int j = 0; j < 8; ++j) sm[(q * 8 + j) * 114 + i] = pp[j];
  }
  __syncthreads();
  for (int e = t; e < 64 * 14; e += blockDim.x) {
    int d = e / 14, q = e % 14;
    uint4 v; u16* pp = (u16*)&v;
#pragma unroll
    for (int j = 0; j < 8; ++j) { int i = q * 8 + j; pp[j] = i < 108 ? sm[d * 114 + i] : (u16)0; }
    *(uint4*)(dst + kb_off(D_, d0 + d, nb * 112 + q * 8)) = v;
  }
}

__device__ __forceinline__ void pack_job(int id, const u16* w1, const u16* w3, const u16* w2, u16* w13p, u16* w2t, u16* sm) {
  if (id < 320) {
    int which = id / 160, r = id % 160;
    pack_w13(which ? w3 : w1, w13p, which, r / 10, (r % 10) * 64, sm);
  } else {
    int r = id - 320;
    pack_w2(w2, w2t, r / 10, (r % 10) * 64, sm);
  }
}

__global__ __launch_bounds__(640) void k_a1(const u16* __restrict__ x, float2* __restrict__ Zt,
                                            const u16* __restrict__ wr, const u16* __restrict__ wi,
                                            unsigned* __restrict__ Wt,
                                            const u16* __restrict__ w1, const u16* __restrict__ w3, const u16* __restrict__ w2,
                                            u16* __restrict__ w13p, u16* __restrict__ w2t) {
  __shared__ float2 buf[2][4][FB];
  __shared__ float red[160];
  const int t = threadIdx.x;
  if (blockIdx.x >= 384) { pack_job(blockIdx.x - 384, w1, w3, w2, w13p, w2t, (u16*)&buf[0][0][0]); return; }
  if (blockIdx.x >= 256) {
    const int s0 = (blockIdx.x - 256) * 16 + (t >= 320 ? 8 : 0);
    for (int k = t % 320; k < DF_; k += 320) {
      unsigned p[8];
#pragma unroll
      for (int r = 0; r < 8; ++r)
        p[r] = (unsigned)wr[(s0 + r) * DF_ + k] | ((unsigned)wi[(s0 + r) * DF_ + k] << 16);
      uint4* dst = (uint4*)(Wt + (size_t)k * S_ + s0);
      dst[0] = make_uint4(p[0], p[1], p[2], p[3]);
      dst[1] = make_uint4(p[4], p[5], p[6], p[7]);
    }
    return;
  }
  const int s0 = blockIdx.x * 8;
  STAMP_DECL STAMP_AT();
  const int row = t / 80, c = t % 80;
  float xv[8];
  {
    uint4 a = *(const uint4*)(x + (size_t)(s0 + row) * D_ + c * 8);
    unsigned u[4] = {a.x, a.y, a.z, a.w};
#pragma unroll
    for (int i = 0; i < 4; ++i) { xv[2 * i] = __uint_as_float(u[i] << 16); xv[2 * i + 1] = __uint_as_float(u[i] & 0xffff0000u); }
  }
  const int f = t / 160, j = t % 160;
  const float2 w2_ = cispi<-1>((float)(j & 3) * (1.0f / 8.0f)), w3_ = cispi<-1>((float)(j & 15) * (1.0f / 32.0f));
  const float2 w4_ = cispi<-1>((float)j * (1.0f / 320.0f));
  STAMP_AT();
  ln8(xv, red, row, c);
  STAMP_AT();
  {
    float* dst = (float*)&buf[0][row >> 1][0] + (row & 1);
#pragma unroll
    for (int i = 0; i < 8; ++i) dst[2 * PD(c * 8 + i)] = xv[i];
  }
  __syncthreads();
  fft640<-1>(buf[0][f], buf[1][f], j, w2_, w3_, w4_);
  __syncthreads();
  STAMP_AT();
#pragma unroll
  for (int i = 0; i < 3; ++i) {
    int e = t + 640 * i;
    if (i < 2 || e < DF_ * 4) {
      int k = e >> 2, ff = e & 3;
      float2 a = buf[0][ff][PD(k)], b = buf[0][ff][PD(k == 0 ? 0 : 640 - k)];
      float4 o;
      o.x = 0.5f * (a.x + b.x); o.y = 0.5f * (a.y - b.y);
      o.z = 0.5f * (a.y + b.y); o.w = 0.5f * (b.x - a.x);
      *(float4*)(Zt + (size_t)k * S_ + s0 + 2 * ff) = o;
    }
  }
  STAMP_AT();
  STAMP_OUT(2);
}

constexpr int CB = 2048 + 256;
__global__ __launch_bounds__(256) void k_a2(float2* __restrict__ Zt, const unsigned* __restrict__ Wt,
                                            const u16* __restrict__ w1, const u16* __restrict__ w3, const u16* __restrict__ w2,
                                            u16* __restrict__ w13p, u16* __restrict__ w2t) {
  __shared__ float2 smem[2 * CB];
  const int t = threadIdx.x;
  if (blockIdx.x >= DF_) {
    pack_job(blockIdx.x - DF_ + 160, w1, w3, w2, w13p, w2t, (u16*)smem);
    return;
  }
  float2* b0 = smem; float2* b1 = smem + CB;
  const int k = blockIdx.x;
  float2* col = Zt + (size_t)k * S_;
  STAMP_DECL STAMP_AT();
  float2 v[8];
#pragma unroll
  for (int r = 0; r < 8; ++r) v[r] = col[t + 256 * r];
  unsigned wv[8];
#pragma unroll
  for (int r = 0; r < 8; ++r) wv[r] = Wt[(size_t)k * S_ + t + 256 * r];
  const float2 wp2 = cispi<-1>((float)(t & 7) * (1.0f / 32.0f));
  const float2 wp3 = cispi<-1>((float)(t & 63) * (1.0f / 256.0f));
  const float2 wp4a = cispi<-1>((float)t * (1.0f / 1024.0f)), wp4b = cispi<-1>((float)(t + 256) * (1.0f / 1024.0f));
  dft8<-1>(v);
  STAMP_AT();
#pragma unroll
  for (int r = 0; r < 8; ++r) b0[PD(8 * t + r)] = v[r];
  __syncthreads();
  {
    int m = t & 7;
#pragma unroll
    for (int r = 0; r < 8; ++r) v[r] = b0[PD(t + 256 * r)];
    twiddle_pow<8>(v, wp2);
    dft8<-1>(v);
#pragma unroll
    for (int r = 0; r < 8; ++r) b1[PD((t >> 3) * 64 + m + 8 * r)] = v[r];
  }
  __syncthreads();
  {
    int m = t & 63;
#pragma unroll
    for (int r = 0; r < 8; ++r) v[r] = b1[PD(t + 256 * r)];
    twiddle_pow<8>(v, wp3);
    dft8<-1>(v);
#pragma unroll
    for (int r = 0; r < 8; ++r) b0[PD((t >> 6) * 512 + m + 64 * r)] = v[r];
  }
  __syncthreads();
#pragma unroll
  for (int h = 0; h < 2; ++h) {
    int j = t + 256 * h;
    float2 a[4] = {b0[PD(j)], b0[PD(j + 512)], b0[PD(j + 1024)], b0[PD(j + 1536)]};
    twiddle_pow<4>(a, h ? wp4b : wp4a);
    dft4<-1>(a[0], a[1], a[2], a[3]);
    v[h] = a[0]; v[h + 2] = a[1]; v[h + 4] = a[2]; v[h + 6] = a[3];
  }
  STAMP_AT();
  const float sc = 1.0f / (2048.0f * 640.0f);
#pragma unroll
  for (int r = 0; r < 8; ++r) {
    float2 w = make_float2(__uint_as_float(wv[r] << 16) * sc, __uint_as_float(wv[r] & 0xffff0000u) * sc);
    v[r] = cmul(v[r], w);
  }
  dft8<1>(v);
#pragma unroll
  for (int r = 0; r < 8; ++r) b1[PD(8 * t + r)] = v[r];
  __syncthreads();
  {
    int m = t & 7;
#pragma unroll
    for (int r = 0; r < 8; ++r) v[r] = b1[PD(t + 256 * r)];
    twiddle_pow<8>(v, twc<1>(wp2));
    dft8<1>(v);
#pragma unroll
    for (int r = 0; r < 8; ++r) b0[PD((t >> 3) * 64 + m + 8 * r)] = v[r];
  }
  __syncthreads();
  {
    int m = t & 63;
#pragma unroll
    for (int r = 0; r < 8; ++r) v[r] = b0[PD(t + 256 * r)];
    twiddle_pow<8>(v, twc<1>(wp3));
    dft8<1>(v);
#pragma unroll
    for (int r = 0; r < 8; ++r) b1[PD((t >> 6) * 512 + m + 64 * r)] = v[r];
  }
  __syncthreads();
#pragma unroll
  for (int h = 0; h < 2; ++h) {
    int j = t + 256 * h;
    float2 a[4] = {b1[PD(j)], b1[PD(j + 512)], b1[PD(j + 1024)], b1[PD(j + 1536)]};
    twiddle_pow<4>(a, twc<1>(h ? wp4b : wp4a));
    dft4<1>(a[0], a[1], a[2], a[3]);
    col[j] = a[0]; col[j + 512] = a[1]; col[j + 1024] = a[2]; col[j + 1536] = a[3];
  }
  STAMP_AT();
  STAMP_OUT(3);
}

__global__ __launch_bounds__(640) void k_a3(const float2* __restrict__ Zt, const u16* xin,
                                            u16* xout, u16* __restrict__ xln,
                                            const u16* __restrict__ w1, const u16* __restrict__ w3, const u16* __restrict__ w2,
                                            u16* __restrict__ w13p, u16* __restrict__ w2t) {
  __shared__ float2 buf[2][4][FB];
  __shared__ float red[160];
  if (blockIdx.x >= 256) { pack_job(blockIdx.x - 256 + 320, w1, w3, w2, w13p, w2t, (u16*)&buf[0][0][0]); return; }
  const int t = threadIdx.x, s0 = blockIdx.x * 8;
  STAMP_DECL STAMP_AT();
  float4 z[3];
#pragma unroll
  for (int i = 0; i < 3; ++i) {
    int e = t + 640 * i;
    if (i < 2 || e < DF_ * 4) z[i] = *(const float4*)(Zt + (size_t)(e >> 2) * S_ + s0 + 2 * (e & 3));
  }
  const int row = t / 80, c = t % 80;
  float xv[8];
  {
    uint4 a = *(const uint4*)(xin + (size_t)(s0 + row) * D_ + c * 8);
    unsigned u[4] = {a.x, a.y, a.z, a.w};
#pragma unroll
    for (int i = 0; i < 4; ++i) { xv[2 * i] = __uint_as_float(u[i] << 16); xv[2 * i + 1] = __uint_as_float(u[i] & 0xffff0000u); }
  }
  const int f = t / 160, j = t % 160;
  const float2 w2_ = cispi<1>((float)(j & 3) * (1.0f / 8.0f)), w3_ = cispi<1>((float)(j & 15) * (1.0f / 32.0f));
  const float2 w4_ = cispi<1>((float)j * (1.0f / 320.0f));
#pragma unroll
  for (int i = 0; i < 3; ++i) {
    int e = t + 640 * i;
    if (i < 2 || e < DF_ * 4) {
      int k = e >> 2, ff = e & 3;
      if (k == 0 || k == 320) {
        buf[0][ff][PD(k)] = make_float2(z[i].x, z[i].z);
      } else {
        buf[0][ff][PD(k)] = make_float2(z[i].x - z[i].w, z[i].y + z[i].z);
        buf[0][ff][PD(640 - k)] = make_float2(z[i].x + z[i].w, z[i].z - z[i].y);
      }
    }
  }
  __syncthreads();
  STAMP_AT();
  fft640<1>(buf[0][f], buf[1][f], j, w2_, w3_, w4_);
  __syncthreads();
  STAMP_AT();
  {
    const float* src = (const float*)&buf[0][row >> 1][0] + (row & 1);
    unsigned u[4];
#pragma unroll
    for (int i = 0; i < 8; ++i) xv[i] = rbf(xv[i] + rbf(src[2 * PD(c * 8 + i)]));
#pragma unroll
    for (int i = 0; i < 4; ++i) u[i] = (__float_as_uint(xv[2 * i]) >> 16) | (__float_as_uint(xv[2 * i + 1]) & 0xffff0000u);
    *(uint4*)(xout + (size_t)(s0 + row) * D_ + c * 8) = make_uint4(u[0], u[1], u[2], u[3]);
  }
  ln8(xv, red, row, c);
  {
    unsigned u[4];
#pragma unroll
    for (int i = 0; i < 4; ++i) u[i] = (__float_as_uint(xv[2 * i]) >> 16) | (__float_as_uint(xv[2 * i + 1]) & 0xffff0000u);
    *(uint4*)(xln + kb_off(S_, s0 + row, c * 8)) = make_uint4(u[0], u[1], u[2], u[3]);
  }
  STAMP_AT();
  STAMP_OUT(4);
}

template <int N> __device__ __forceinline__ void wait_vm() { asm volatile("s_waitcnt vmcnt(%0)" ::"i"(N) : "memory"); }
__device__ __forceinline__ void wait_lgkm0() { asm volatile("s_waitcnt lgkmcnt(0)" ::: "memory"); }

constexpr int FP_ = 1792;

template <int NP, int L>
__device__ __forceinline__ void g1_body(const u16* __restrict__ A, const u16* __restrict__ Bt, u16* __restrict__ out,
                                        char* lds, int bm, int bn, int wm, int p0) {
  constexpr int BM = 128, BN = 224, NS = 6, NT = 20, PF = 5;
  constexpr int ABYTES = BM * 64, BBYTES = BN * 64, STG = ABYTES + BBYTES;
  const int t = threadIdx.x, lane = t & 63, fr = lane & 15, fq = lane >> 4;
  const char* Ag = (const char*)A + (size_t)bm * ABYTES + t * 16;
  const char* Bg = (const char*)Bt + (size_t)bn * BBYTES + t * 16;
  auto stage = [&](int ks) {
    char* la = lds + (ks % NS) * STG + t * 16;
    __builtin_amdgcn_global_load_lds((const unsigned*)(Ag + (size_t)ks * (S_ * 64)), (unsigned*)la, 16, 0, 0);
    __builtin_amdgcn_global_load_lds((const unsigned*)(Bg + (size_t)ks * (3584 * 64)), (unsigned*)(la + ABYTES), 16, 0, 0);
    if (L == 3)
      __builtin_amdgcn_global_load_lds((const unsigned*)(Bg + (size_t)ks * (3584 * 64) + 8192), (unsigned*)(la + ABYTES + 8192), 16, 0, 0);
  };
  const int aoff = (wm * 32 + fr) * 64 + ((fq ^ swz(fr)) << 4);
  const int boff = ABYTES + (p0 * 32 + fr) * 64 + ((fq ^ swz(fr)) << 4);
  bf16x8 af[2][2], bfr[2][2 * NP];
  auto ldfr = [&](int set, int ks) {
    const char* l = lds + (ks % NS) * STG;
#pragma unroll
    for (int m = 0; m < 2; ++m) af[set][m] = *(const bf16x8*)(l + aoff + m * 1024);
#pragma unroll
    for (int n = 0; n < 2 * NP; ++n) bfr[set][n] = *(const bf16x8*)(l + boff + n * 1024);
  };
  f32x4 acc[2][2 * NP];
#pragma unroll
  for (int m = 0; m < 2; ++m)
#pragma unroll
    for (int n = 0; n < 2 * NP; ++n) acc[m][n] = (f32x4){0.f, 0.f, 0.f, 0.f};
  STAMP_DECL STAMP_AT();
#pragma unroll
  for (int i = 0; i < PF; ++i) stage(i);
  wait_vm<(PF - 1) * L>();
  __builtin_amdgcn_s_barrier();
  ldfr(0, 0);
#pragma unroll
  for (int ks = 0; ks < NT; ++ks) {
    STAMP_AT();
    const int cur = ks & 1;
    if (ks + 1 < NT) {
      constexpr int dummy = 0; (void)dummy;
      const int issued = (ks + PF - 1 < NT - 1) ? ks + PF - 1 : NT - 1;
      const int allow = (issued - (ks + 1)) * L;
      if (allow >= 3 * L) wait_vm<3 * L>(); else if (allow == 2 * L) wait_vm<2 * L>(); else if (allow == L) wait_vm<L>(); else wait_vm<0>();
      __builtin_amdgcn_s_barrier();
      if (ks + PF < NT) stage(ks + PF);
      ldfr(cur ^ 1, ks + 1);
    }
#pragma unroll
    for (int n = 0; n < 2 * NP; ++n)
#pragma unroll
      for (int m = 0; m < 2; ++m)
        acc[m][n] = __builtin_amdgcn_mfma_f32_16x16x32_bf16(bfr[cur][n], af[cur][m], acc[m][n], 0, 0, 0);
    if (ks + 1 < NT) {
#pragma unroll
      for (int g = 0; g < L; ++g) {
        __builtin_amdgcn_sched_group_barrier(0x8, 1, 0);
        __builtin_amdgcn_sched_group_barrier(0x10, 1, 0);
      }
#pragma unroll
      for (int g = 0; g < 2 * NP + 2; ++g) {
        __builtin_amdgcn_sched_group_barrier(0x8, 1, 0);
        __builtin_amdgcn_sched_group_barrier(0x100, 1, 0);
      }
    }
    __builtin_amdgcn_sched_barrier(0);
  }
  STAMP_AT();
#pragma unroll
  for (int m = 0; m < 2; ++m) {
    int row = bm * BM + wm * 32 + m * 16 + fr;
#pragma unroll
    for (int p = 0; p < NP; ++p) {
      int hc = bn * 112 + (p0 + p) * 16 + fq * 4;
      u16 o[4];
#pragma unroll
      for (int j = 0; j < 4; ++j) {
        float g = rbf(acc[m][2 * p][j]), u = rbf(acc[m][2 * p + 1][j]);
        float sg = rbf(g * __builtin_amdgcn_rcpf(1.0f + __expf(-g)));
        o[j] = f2bf(sg * u);
      }
      *(uint2*)(out + kb_off(S_, row, hc)) = make_uint2(o[0] | ((unsigned)o[1] << 16), o[2] | ((unsigned)o[3] << 16));
    }
  }
  STAMP_AT();
  STAMP_OUT(0);
}
__global__ __launch_bounds__(512) void k_g1(const u16* __restrict__ A, const u16* __restrict__ Bt, u16* __restrict__ out) {
  __shared__ __attribute__((aligned(16))) char lds[6 * (128 + 224) * 64];
  const int wid = __builtin_amdgcn_readfirstlane(threadIdx.x >> 6);
  const int xcd = blockIdx.x & 7, idx = blockIdx.x >> 3;
  const int bm = (xcd >> 1) * 4 + (idx & 3), bn = (xcd & 1) * 8 + (idx >> 2);
  if (wid < 4) g1_body<4, 3>(A, Bt, out, lds, bm, bn, wid, 0);
  else if (wid < 6) g1_body<3, 3>(A, Bt, out, lds, bm, bn, wid - 4, 4);
  else g1_body<3, 2>(A, Bt, out, lds, bm, bn, wid - 4, 4);
}

template <int L>
__device__ __forceinline__ void g2_body(const u16* __restrict__ A, const u16* __restrict__ Bt, const u16* res, u16* out,
                                        char* lds, int bm, int bn, int wid) {
  constexpr int BM = 64, BN = 80, NS = 4, NT = 14, PF = 3;
  constexpr int ABYTES = 4 * BM * 64, BBYTES = 4 * BN * 64, STG = ABYTES + BBYTES;
  const int t = threadIdx.x, lane = t & 63, fr = lane & 15, fq = lane >> 4;
  const int ks4 = wid & 3, mh = wid >> 2;
  const char* Ag[2]; const char* Bg[3];
#pragma unroll
  for (int i = 0; i < 2; ++i) { int by = t * 16 + i * 8192; Ag[i] = (const char*)A + (size_t)(by >> 12) * (S_ * 64) + (size_t)bm * 4096 + (by & 4095); }
#pragma unroll
  for (int i = 0; i < 3; ++i) { int by = t * 16 + i * 8192; int kb = by / 5120; Bg[i] = (const char*)Bt + (size_t)kb * (D_ * 64) + (size_t)bn * 5120 + (by - kb * 5120); }
  auto stage = [&](int st) {
    char* la = lds + (st % NS) * STG + t * 16;
#pragma unroll
    for (int i = 0; i < 2; ++i)
      __builtin_amdgcn_global_load_lds((const unsigned*)(Ag[i] + (size_t)st * (4 * S_ * 64)), (unsigned*)(la + i * 8192), 16, 0, 0);
#pragma unroll
    for (int i = 0; i < L - 2; ++i)
      __builtin_amdgcn_global_load_lds((const unsigned*)(Bg[i] + (size_t)st * (4 * D_ * 64)), (unsigned*)(la + ABYTES + i * 8192), 16, 0, 0);
  };
  const int aoff = ks4 * 4096 + (mh * 32 + fr) * 64 + ((fq ^ swz(fr)) << 4);
  const int boff = ABYTES + ks4 * 5120 + fr * 64 + ((fq ^ swz(fr)) << 4);
  bf16x8 af[2][2], bfr[2][5];
  auto ldfr = [&](int set, int st) {
    const char* l = lds + (st % NS) * STG;
#pragma unroll
    for (int m = 0; m < 2; ++m) af[set][m] = *(const bf16x8*)(l + aoff + m * 1024);
#pragma unroll
    for (int n = 0; n < 5; ++n) bfr[set][n] = *(const bf16x8*)(l + boff + n * 1024);
  };
  f32x4 acc[2][5];
#pragma unroll
  for (int m = 0; m < 2; ++m)
#pragma unroll
    for (int n = 0; n < 5; ++n) acc[m][n] = (f32x4){0.f, 0.f, 0.f, 0.f};
  STAMP_DECL STAMP_AT();
  uint2 rvp[3];
#pragma unroll
  for (int i = 0; i < 3; ++i) {
    int fi = wid + 8 * i; if (fi >= 20) fi = wid;
    rvp[i] = *(const uint2*)(res + (size_t)(bm * BM + (fi / 5) * 16 + fr) * D_ + bn * BN + (fi % 5) * 16 + fq * 4);
  }
#pragma unroll
  for (int i = 0; i < PF; ++i) stage(i);
  wait_vm<(PF - 1) * L>();
  __builtin_amdgcn_s_barrier();
  ldfr(0, 0);
#pragma unroll
  for (int st = 0; st < NT; ++st) {
    STAMP_AT();
    const int cur = st & 1;
    if (st + 1 < NT) {
      const int issued = (st + PF - 1 < NT - 1) ? st + PF - 1 : NT - 1;
      const int allow = (issued - (st + 1)) * L;
      if (allow >= L) wait_vm<L>(); else wait_vm<0>();
      __builtin_amdgcn_s_barrier();
      if (st + PF < NT) stage(st + PF);
      ldfr(cur ^ 1, st + 1);
    }
#pragma unroll
    for (int m = 0; m < 2; ++m)
#pragma unroll
      for (int n = 0; n < 5; ++n)
        acc[m][n] = __builtin_amdgcn_mfma_f32_16x16x32_bf16(bfr[cur][n], af[cur][m], acc[m][n], 0, 0, 0);
    if (st + 1 < NT) {
#pragma unroll
      for (int g = 0; g < 5; ++g) {
        __builtin_amdgcn_sched_group_barrier(0x8, 1, 0);
        __builtin_amdgcn_sched_group_barrier(0x10, 1, 0);
        __builtin_amdgcn_sched_group_barrier(0x100, 1, 0);
      }
#pragma unroll
      for (int g = 0; g < 2; ++g) {
        __builtin_amdgcn_sched_group_barrier(0x8, 1, 0);
        __builtin_amdgcn_sched_group_barrier(0x100, 1, 0);
      }
    }
    __builtin_amdgcn_sched_barrier(0);
  }
  STAMP_AT();
  __syncthreads();
  f32x4* red = (f32x4*)lds;
#pragma unroll
  for (int m = 0; m < 2; ++m)
#pragma unroll
    for (int n = 0; n < 5; ++n) red[(ks4 * 20 + mh * 10 + m * 5 + n) * 64 + lane] = acc[m][n];
  __syncthreads();
#pragma unroll
  for (int i = 0; i < 3; ++i) {
    int fi = wid + 8 * i;
    if (fi < 20) {
      f32x4 s = red[fi * 64 + lane] + red[(20 + fi) * 64 + lane] + red[(40 + fi) * 64 + lane] + red[(60 + fi) * 64 + lane];
      int mrow = fi / 5, n = fi % 5;
      int row = bm * BM + mrow * 16 + fr;
      int cc = bn * BN + n * 16 + fq * 4;
      uint2 rv = rvp[i];
      float r0 = __uint_as_float(rv.x << 16), r1 = __uint_as_float(rv.x & 0xffff0000u);
      float r2 = __uint_as_float(rv.y << 16), r3 = __uint_as_float(rv.y & 0xffff0000u);
      u16 o0 = f2bf(r0 + rbf(s[0])), o1 = f2bf(r1 + rbf(s[1]));
      u16 o2 = f2bf(r2 + rbf(s[2])), o3 = f2bf(r3 + rbf(s[3]));
      *(uint2*)(out + (size_t)row * D_ + cc) = make_uint2(o0 | ((unsigned)o1 << 16), o2 | ((unsigned)o3 << 16));
    }
  }
  STAMP_AT();
  STAMP_OUT(1);
}
__global__ __launch_bounds__(512) void k_g2(const u16* __restrict__ A, const u16* __restrict__ Bt, const u16* res, u16* out) {
  __shared__ __attribute__((aligned(16))) char lds[4 * 4 * (64 + 80) * 64];
  const int wid = __builtin_amdgcn_readfirstlane(threadIdx.x >> 6);
  const int xcd = blockIdx.x & 7, idx = blockIdx.x >> 3;
  const int bm = xcd * 4 + (idx & 3), bn = idx >> 2;
  if (wid < 4) g2_body<5>(A, Bt, res, out, lds, bm, bn, wid);
  else g2_body<4>(A, Bt, res, out, lds, bm, bn, wid);
}

extern "C" void kernel_launch(void* const* d_in, const int* in_sizes, int n_in,
                              void* d_out, int out_size, void* d_ws, size_t ws_size,
                              hipStream_t stream) {
  const u16* Ax = (const u16*)d_in[0];
  const u16* wr = (const u16*)d_in[1];
  const u16* wi = (const u16*)d_in[2];
  const u16* w1 = (const u16*)d_in[4];
  const u16* w3 = (const u16*)d_in[5];
  const u16* w2 = (const u16*)d_in[6];
  char* ws = (char*)d_ws;
  size_t off = 0;
  auto alloc = [&](size_t bytes) { void* p = ws + off; off += (bytes + 255) & ~(size_t)255; return p; };
  float2* Zt = (float2*)alloc((size_t)DF_ * S_ * 8);
  unsigned* Wt = (unsigned*)alloc((size_t)DF_ * S_ * 4);
  u16* xb = (u16*)alloc((size_t)S_ * D_ * 2);
  u16* xln = (u16*)alloc((size_t)S_ * D_ * 2);
  u16* h = (u16*)alloc((size_t)S_ * 1792 * 2);
  u16* w13p = (u16*)alloc((size_t)16 * 224 * D_ * 2);
  u16* w2t = (u16*)alloc((size_t)D_ * 1792 * 2);
  u16* outp = (u16*)d_out;

  for (int layer = 0; layer < 2; ++layer) {
    const u16* xin = layer == 0 ? Ax : xb;
    k_a1<<<layer == 0 ? 384 + 160 : 256, 640, 0, stream>>>(xin, Zt, wr, wi, Wt, w1, w3, w2, w13p, w2t);
    k_a2<<<layer == 0 ? DF_ + 160 : DF_, 256, 0, stream>>>(Zt, Wt, w1, w3, w2, w13p, w2t);
    k_a3<<<layer == 0 ? 256 + 160 : 256, 640, 0, stream>>>(Zt, xin, xb, xln, w1, w3, w2, w13p, w2t);
    k_g1<<<256, 512, 0, stream>>>(xln, w13p, h);
    k_g2<<<256, 512, 0, stream>>>(h, w2t, xb, layer == 0 ? xb : outp);
  }
}
```

```cpp
#include <hip/hip_runtime.h>
#include <hip/hip_bf16.h>
#include <stdint.h>

typedef unsigned short u16;
using bf16x8 = __attribute__((ext_vector_type(8))) short;
using f32x4 = __attribute__((ext_vector_type(4))) float;

constexpr int S_ = 2048, D_ = 640, DF_ = 321, F_ = 1728;

__device__ __forceinline__ float bf2f(u16 v) { return __uint_as_float(((unsigned)v) << 16); }
__device__ __forceinline__ u16 f2bf(float f) { return __bfloat16_as_ushort(__float2bfloat16(f)); }
__device__ __forceinline__ float rbf(float f) { return bf2f(f2bf(f)); }

__device__ __forceinline__ float2 cadd(float2 a, float2 b) { return make_float2(a.x + b.x, a.y + b.y); }
__device__ __forceinline__ float2 csub(float2 a, float2 b) { return make_float2(a.x - b.x, a.y - b.y); }
__device__ __forceinline__ float2 cmul(float2 a, float2 b) {
  return make_float2(a.x * b.x - a.y * b.y, a.x * b.y + a.y * b.x);
}
template <int SG> __device__ __forceinline__ float2 mul_i(float2 a) {
  return SG > 0 ? make_float2(-a.y, a.x) : make_float2(a.y, -a.x);
}
template <int SG> __device__ __forceinline__ float2 twc(float2 w) {
  return SG > 0 ? make_float2(w.x, -w.y) : w;
}

template <int SG> __device__ __forceinline__ void dft4(float2& a0, float2& a1, float2& a2, float2& a3) {
  float2 s0 = cadd(a0, a2), d0 = csub(a0, a2), s1 = cadd(a1, a3), d1 = mul_i<SG>(csub(a1, a3));
  a0 = cadd(s0, s1); a2 = csub(s0, s1); a1 = cadd(d0, d1); a3 = csub(d0, d1);
}
template <int SG> __device__ __forceinline__ void dft8(float2* v) {
  float2 e0 = v[0], e1 = v[2], e2 = v[4], e3 = v[6], o0 = v[1], o1 = v[3], o2 = v[5], o3 = v[7];
  dft4<SG>(e0, e1, e2, e3); dft4<SG>(o0, o1, o2, o3);
  const float h = 0.70710678118654752440f;
  float2 t1 = make_float2(h * (o1.x - SG * o1.y), h * (o1.y + SG * o1.x));
  float2 t2 = mul_i<SG>(o2);
  float2 t3 = make_float2(h * (-o3.x - SG * o3.y), h * (-o3.y + SG * o3.x));
  v[0] = cadd(e0, o0); v[4] = csub(e0, o0);
  v[1] = cadd(e1, t1); v[5] = csub(e1, t1);
  v[2] = cadd(e2, t2); v[6] = csub(e2, t2);
  v[3] = cadd(e3, t3); v[7] = csub(e3, t3);
}
template <int SG> __device__ __forceinline__ void dft5(float2& a0, float2& a1, float2& a2, float2& a3, float2& a4) {
  const float c1 = 0.30901699437494742410f, c2 = -0.80901699437494742410f;
  const float s1 = 0.95105651629515357212f, s2 = 0.58778525229247312917f;
  float2 p1 = cadd(a1, a4), m1 = csub(a1, a4), p2 = cadd(a2, a3), m2 = csub(a2, a3);
  float2 t1 = make_float2(a0.x + c1 * p1.x + c2 * p2.x, a0.y + c1 * p1.y + c2 * p2.y);
  float2 t2 = make_float2(a0.x + c2 * p1.x + c1 * p2.x, a0.y + c2 * p1.y + c1 * p2.y);
  float2 u1 = mul_i<SG>(make_float2(s1 * m1.x + s2 * m2.x, s1 * m1.y + s2 * m2.y));
  float2 u2 = mul_i<SG>(make_float2(s2 * m1.x - s1 * m2.x, s2 * m1.y - s1 * m2.y));
  a0 = cadd(a0, cadd(p1, p2));
  a1 = cadd(t1, u1); a4 = csub(t1, u1);
  a2 = cadd(t2, u2); a3 = csub(t2, u2);
}
template <int SG> __device__ __forceinline__ void dft10(float2* v) {
  float2 e0 = v[0], e1 = v[2], e2 = v[4], e3 = v[6], e4 = v[8];
  float2 o0 = v[1], o1 = v[3], o2 = v[5], o3 = v[7], o4 = v[9];
  dft5<SG>(e0, e1, e2, e3, e4); dft5<SG>(o0, o1, o2, o3, o4);
  const float c1 = 0.80901699437494742410f, s1 = 0.58778525229247312917f;
  const float c2 = 0.30901699437494742410f, s2 = 0.95105651629515357212f;
  o1 = cmul(o1, make_float2(c1, SG * s1));
  o2 = cmul(o2, make_float2(c2, SG * s2));
  o3 = cmul(o3, make_float2(-c2, SG * s2));
  o4 = cmul(o4, make_float2(-c1, SG * s1));
  v[0] = cadd(e0, o0); v[5] = csub(e0, o0);
  v[1] = cadd(e1, o1); v[6] = csub(e1, o1);
  v[2] = cadd(e2, o2); v[7] = csub(e2, o2);
  v[3] = cadd(e3, o3); v[8] = csub(e3, o3);
  v[4] = cadd(e4, o4); v[9] = csub(e4, o4);
}

__device__ __forceinline__ int PD(int i) { return i + (i >> 3); }
typedef float f32x4v __attribute__((ext_vector_type(4)));
typedef float f32x2v __attribute__((ext_vector_type(2)));
typedef unsigned u32x4v __attribute__((ext_vector_type(4)));
typedef unsigned u32x2v __attribute__((ext_vector_type(2)));
#define ST4F(p, v) (*(float4*)(p) = (v))
#define ST2F(p, v) (*(float2*)(p) = (v))
#define ST4U(p, v) (*(uint4*)(p) = (v))
#define ST2U(p, v) (*(uint2*)(p) = (v))
__device__ __forceinline__ int swz(int row) { return (-(row >> 2)) & 3; }
__device__ __forceinline__ size_t kb_off(int R, int row, int k) {
  return (size_t)(k >> 5) * ((size_t)R * 32) + (size_t)row * 32 + ((((k >> 3) & 3) ^ swz(row)) << 3) + (k & 7);
}

template <int SG> __device__ __forceinline__ float2 cispi(float x) {
  float sn, cs; sincospif(x, &sn, &cs);
  return make_float2(cs, SG * sn);
}
template <int R> __device__ __forceinline__ void twiddle_pow(float2* v, float2 w1) {
  float2 w2 = cmul(w1, w1), w3 = cmul(w2, w1), w4 = cmul(w2, w2);
  v[1] = cmul(v[1], w1); v[2] = cmul(v[2], w2); v[3] = cmul(v[3], w3);
  if (R > 4) {
    float2 w5 = cmul(w4, w1), w6 = cmul(w3, w3), w7 = cmul(w4, w3);
    v[4] = cmul(v[4], w4); v[5] = cmul(v[5], w5); v[6] = cmul(v[6], w6); v[7] = cmul(v[7], w7);
    if (R > 8) { float2 w8 = cmul(w4, w4), w9 = cmul(w8, w1); v[8] = cmul(v[8], w8); v[9] = cmul(v[9], w9); }
  }
}

template <int SG>
__device__ __forceinline__ void fft640(float2* b0, float2* b1, int j, float2 w2_, float2 w3_, float2 w4_) {
  float2 v[10];
#pragma unroll
  for (int r = 0; r < 4; ++r) v[r] = b0[PD(j + 160 * r)];
  dft4<SG>(v[0], v[1], v[2], v[3]);
#pragma unroll
  for (int r = 0; r < 4; ++r) b1[PD(4 * j + r)] = v[r];
  __syncthreads();
#pragma unroll
  for (int r = 0; r < 4; ++r) v[r] = b1[PD(j + 160 * r)];
  twiddle_pow<4>(v, w2_);
  dft4<SG>(v[0], v[1], v[2], v[3]);
#pragma unroll
  for (int r = 0; r < 4; ++r) b0[PD((j >> 2) * 16 + (j & 3) + 4 * r)] = v[r];
  __syncthreads();
#pragma unroll
  for (int r = 0; r < 4; ++r) v[r] = b0[PD(j + 160 * r)];
  twiddle_pow<4>(v, w3_);
  dft4<SG>(v[0], v[1], v[2], v[3]);
#pragma unroll
  for (int r = 0; r < 4; ++r) b1[PD((j >> 4) * 64 + (j & 15) + 16 * r)] = v[r];
  __syncthreads();
  if (j < 64) {
#pragma unroll
    for (int r = 0; r < 10; ++r) v[r] = b1[PD(j + 64 * r)];
    twiddle_pow<10>(v, w4_);
    dft10<SG>(v);
#pragma unroll
    for (int r = 0; r < 10; ++r) b0[PD(j + 64 * r)] = v[r];
  }
}

__device__ __forceinline__ void ln8(float* xv, float* red, int row, int c) {
  float s = 0.f;
#pragma unroll
  for (int i = 0; i < 8; ++i) s += xv[i];
  s += __shfl_xor(s, 1); s += __shfl_xor(s, 2); s += __shfl_xor(s, 4);
  if ((c & 7) == 0) red[row * 10 + (c >> 3)] = s;
  __syncthreads();
  float mean = 0.f;
#pragma unroll
  for (int i = 0; i < 10; ++i) mean += red[row * 10 + i];
  mean *= (1.0f / 640.0f);
  float q = 0.f;
#pragma unroll
  for (int i = 0; i < 8; ++i) { float d = xv[i] - mean; q += d * d; }
  q += __shfl_xor(q, 1); q += __shfl_xor(q, 2); q += __shfl_xor(q, 4);
  if ((c & 7) == 0) red[80 + row * 10 + (c >> 3)] = q;
  __syncthreads();
  float var = 0.f;
#pragma unroll
  for (int i = 0; i < 10; ++i) var += red[80 + row * 10 + i];
  var *= (1.0f / 640.0f);
  float mb = rbf(mean), vb = rbf(var);
  float den = rbf(sqrtf(rbf(vb + 1e-5f)));
  float inv = 1.0f / den;
#pragma unroll
  for (int i = 0; i < 8; ++i) xv[i] = rbf(rbf(xv[i] - mb) * inv);
}

#define STAMP_DECL
#define STAMP_AT()
#define STAMP_OUT(w)
constexpr int FB = 720;

__device__ __forceinline__ void pack_w13(const u16* __restrict__ src, u16* __restrict__ dst, int which, int nb, int k0, u16* sm) {
  const int t = threadIdx.x;
  for (int e = t; e < 64 * 27; e += blockDim.x) {
    int r = e / 27, q = e % 27;
    uint2 v = *(const uint2*)(src + (size_t)(k0 + r) * F_ + nb * 108 + q * 4);
    sm[(q * 4 + 0) * 66 + r] = (u16)(v.x & 0xffff); sm[(q * 4 + 1) * 66 + r] = (u16)(v.x >> 16);
    sm[(q * 4 + 2) * 66 + r] = (u16)(v.y & 0xffff); sm[(q * 4 + 3) * 66 + r] = (u16)(v.y >> 16);
  }
  __syncthreads();
  for (int e = t; e < 112 * 8; e += blockDim.x) {
    int c = e >> 3, q = e & 7;
    uint4 v = make_uint4(0, 0, 0, 0);
    if (c < 108) {
      u16* pp = (u16*)&v;
#pragma unroll
      for (int i = 0; i < 8; ++i) pp[i] = sm[c * 66 + q * 8 + i];
    }
    int drow = nb * 224 + (c >> 4) * 32 + which * 16 + (c & 15);
    *(uint4*)(dst + kb_off(16 * 224, drow, k0 + q * 8)) = v;
  }
}
__device__ __forceinline__ void pack_w2(const u16* __restrict__ src, u16* __restrict__ dst, int nb, int d0, u16* sm) {
  const int t = threadIdx.x;
  for (int e = t; e < 108 * 8; e += blockDim.x) {
    int i = e >> 3, q = e & 7;
    uint4 v = *(const uint4*)(src + (size_t)(nb * 108 + i) * D_ + d0 + q * 8);
    u16* pp = (u16*)&v;
#pragma unroll
    for (int j = 0; j < 8; ++j) sm[(q * 8 + j) * 114 + i] = pp[j];
  }
  __syncthreads();
  for (int e = t; e < 64 * 14; e += blockDim.x) {
    int d = e / 14, q = e % 14;
    uint4 v; u16* pp = (u16*)&v;
#pragma unroll
    for (int j = 0; j < 8; ++j) { int i = q * 8 + j; pp[j] = i < 108 ? sm[d * 114 + i] : (u16)0; }
    *(uint4*)(dst + kb_off(D_, d0 + d, nb * 112 + q * 8)) = v;
  }
}

__device__ __forceinline__ void pack_job(int id, const u16* w1, const u16* w3, const u16* w2, u16* w13p, u16* w2t, u16* sm) {
  if (id < 320) {
    int which = id / 160, r = id % 160;
    pack_w13(which ? w3 : w1, w13p, which, r / 10, (r % 10) * 64, sm);
  } else {
    int r = id - 320;
    pack_w2(w2, w2t, r / 10, (r % 10) * 64, sm);
  }
}

__global__ __launch_bounds__(640) void k_a1(const u16* __restrict__ x, float2* __restrict__ Zt,
                                            const u16* __restrict__ wr, const u16* __restrict__ wi,
                                            unsigned* __restrict__ Wt,
                                            const u16* __restrict__ w1, const u16* __restrict__ w3, const u16* __restrict__ w2,
                                            u16* __restrict__ w13p, u16* __restrict__ w2t) {
  __shared__ float2 buf[2][4][FB];
  __shared__ float red[160];
  const int t = threadIdx.x;
  if (blockIdx.x >= 384) { pack_job(blockIdx.x - 384, w1, w3, w2, w13p, w2t, (u16*)&buf[0][0][0]); return; }
  if (blockIdx.x >= 256) {
    const int s0 = (blockIdx.x - 256) * 16 + (t >= 320 ? 8 : 0);
    for (int k = t % 320; k < DF_; k += 320) {
      unsigned p[8];
#pragma unroll
      for (int r = 0; r < 8; ++r)
        p[r] = (unsigned)wr[(s0 + r) * DF_ + k] | ((unsigned)wi[(s0 + r) * DF_ + k] << 16);
      uint4* dst = (uint4*)(Wt + (size_t)k * S_ + s0);
      dst[0] = make_uint4(p[0], p[1], p[2], p[3]);
      dst[1] = make_uint4(p[4], p[5], p[6], p[7]);
    }
    return;
  }
  const int s0 = blockIdx.x * 8;
  STAMP_DECL STAMP_AT();
  const int row = t / 80, c = t % 80;
  float xv[8];
  {
    uint4 a = *(const uint4*)(x + (size_t)(s0 + row) * D_ + c * 8);
    unsigned u[4] = {a.x, a.y, a.z, a.w};
#pragma unroll
    for (int i = 0; i < 4; ++i) { xv[2 * i] = __uint_as_float(u[i] << 16); xv[2 * i + 1] = __uint_as_float(u[i] & 0xffff0000u); }
  }
  const int f = t / 160, j = t % 160;
  const float2 w2_ = cispi<-1>((float)(j & 3) * (1.0f / 8.0f)), w3_ = cispi<-1>((float)(j & 15) * (1.0f / 32.0f));
  const float2 w4_ = cispi<-1>((float)j * (1.0f / 320.0f));
  STAMP_AT();
  ln8(xv, red, row, c);
  STAMP_AT();
  {
    float* dst = (float*)&buf[0][row >> 1][0] + (row & 1);
#pragma unroll
    for (int i = 0; i < 8; ++i) dst[2 * PD(c * 8 + i)] = xv[i];
  }
  __syncthreads();
  fft640<-1>(buf[0][f], buf[1][f], j, w2_, w3_, w4_);
  __syncthreads();
  STAMP_AT();
#pragma unroll
  for (int i = 0; i < 3; ++i) {
    int e = t + 640 * i;
    if (i < 2 || e < DF_ * 4) {
      int k = e >> 2, ff = e & 3;
      float2 a = buf[0][ff][PD(k)], b = buf[0][ff][PD(k == 0 ? 0 : 640 - k)];
      float4 o;
      o.x = 0.5f * (a.x + b.x); o.y = 0.5f * (a.y - b.y);
      o.z = 0.5f * (a.y + b.y); o.w = 0.5f * (b.x - a.x);
      ST4F(Zt + (size_t)k * S_ + s0 + 2 * ff, o);
    }
  }
  STAMP_AT();
  STAMP_OUT(2);
}

constexpr int CB = 2048 + 256;
__global__ __launch_bounds__(256) void k_a2(float2* __restrict__ Zt, const unsigned* __restrict__ Wt,
                                            const u16* __restrict__ w1, const u16* __restrict__ w3, const u16* __restrict__ w2,
                                            u16* __restrict__ w13p, u16* __restrict__ w2t) {
  __shared__ float2 smem[2 * CB];
  const int t = threadIdx.x;
  if (blockIdx.x >= DF_) {
    pack_job(blockIdx.x - DF_ + 160, w1, w3, w2, w13p, w2t, (u16*)smem);
    return;
  }
  float2* b0 = smem; float2* b1 = smem + CB;
  const int k = blockIdx.x;
  float2* col = Zt + (size_t)k * S_;
  STAMP_DECL STAMP_AT();
  float2 v[8];
#pragma unroll
  for (int r = 0; r < 8; ++r) v[r] = col[t + 256 * r];
  unsigned wv[8];
#pragma unroll
  for (int r = 0; r < 8; ++r) wv[r] = Wt[(size_t)k * S_ + t + 256 * r];
  const float2 wp2 = cispi<-1>((float)(t & 7) * (1.0f / 32.0f));
  const float2 wp3 = cispi<-1>((float)(t & 63) * (1.0f / 256.0f));
  const float2 wp4a = cispi<-1>((float)t * (1.0f / 1024.0f)), wp4b = cispi<-1>((float)(t + 256) * (1.0f / 1024.0f));
  dft8<-1>(v);
  STAMP_AT();
#pragma unroll
  for (int r = 0; r < 8; ++r) b0[PD(8 * t + r)] = v[r];
  __syncthreads();
  {
    int m = t & 7;
#pragma unroll
    for (int r = 0; r < 8; ++r) v[r] = b0[PD(t + 256 * r)];
    twiddle_pow<8>(v, wp2);
    dft8<-1>(v);
#pragma unroll
    for (int r = 0; r < 8; ++r) b1[PD((t >> 3) * 64 + m + 8 * r)] = v[r];
  }
  __syncthreads();
  {
    int m = t & 63;
#pragma unroll
    for (int r = 0; r < 8; ++r) v[r] = b1[PD(t + 256 * r)];
    twiddle_pow<8>(v, wp3);
    dft8<-1>(v);
#pragma unroll
    for (int r = 0; r < 8; ++r) b0[PD((t >> 6) * 512 + m + 64 * r)] = v[r];
  }
  __syncthreads();
#pragma unroll
  for (int h = 0; h < 2; ++h) {
    int j = t + 256 * h;
    float2 a[4] = {b0[PD(j)], b0[PD(j + 512)], b0[PD(j + 1024)], b0[PD(j + 1536)]};
    twiddle_pow<4>(a, h ? wp4b : wp4a);
    dft4<-1>(a[0], a[1], a[2], a[3]);
    v[h] = a[0]; v[h + 2] = a[1]; v[h + 4] = a[2]; v[h + 6] = a[3];
  }
  STAMP_AT();
  const float sc = 1.0f / (2048.0f * 640.0f);
#pragma unroll
  for (int r = 0; r < 8; ++r) {
    float2 w = make_float2(__uint_as_float(wv[r] << 16) * sc, __uint_as_float(wv[r] & 0xffff0000u) * sc);
    v[r] = cmul(v[r], w);
  }
  dft8<1>(v);
#pragma unroll
  for (int r = 0; r < 8; ++r) b1[PD(8 * t + r)] = v[r];
  __syncthreads();
  {
    int m = t & 7;
#pragma unroll
    for (int r = 0; r < 8; ++r) v[r] = b1[PD(t + 256 * r)];
    twiddle_pow<8>(v, twc<1>(wp2));
    dft8<1>(v);
#pragma unroll
    for (int r = 0; r < 8; ++r) b0[PD((t >> 3) * 64 + m + 8 * r)] = v[r];
  }
  __syncthreads();
  {
    int m = t & 63;
#pragma unroll
    for (int r = 0; r < 8; ++r) v[r] = b0[PD(t + 256 * r)];
    twiddle_pow<8>(v, twc<1>(wp3));
    dft8<1>(v);
#pragma unroll
    for (int r = 0; r < 8; ++r) b1[PD((t >> 6) * 512 + m + 64 * r)] = v[r];
  }
  __syncthreads();
#pragma unroll
  for (int h = 0; h < 2; ++h) {
    int j = t + 256 * h;
    float2 a[4] = {b1[PD(j)], b1[PD(j + 512)], b1[PD(j + 1024)], b1[PD(j + 1536)]};
    twiddle_pow<4>(a, twc<1>(h ? wp4b : wp4a));
    dft4<1>(a[0], a[1], a[2], a[3]);
    ST2F(col + j, a[0]); ST2F(col + j + 512, a[1]); ST2F(col + j + 1024, a[2]); ST2F(col + j + 1536, a[3]);
  }
  STAMP_AT();
  STAMP_OUT(3);
}

__global__ __launch_bounds__(640) void k_a3(const float2* __restrict__ Zt, const u16* xin,
                                            u16* xout, u16* __restrict__ xln,
                                            const u16* __restrict__ w1, const u16* __restrict__ w3, const u16* __restrict__ w2,
                                            u16* __restrict__ w13p, u16* __restrict__ w2t) {
  __shared__ float2 buf[2][4][FB];
  __shared__ float red[160];
  if (blockIdx.x >= 256) { pack_job(blockIdx.x - 256 + 320, w1, w3, w2, w13p, w2t, (u16*)&buf[0][0][0]); return; }
  const int t = threadIdx.x, s0 = blockIdx.x * 8;
  STAMP_DECL STAMP_AT();
  float4 z[3];
#pragma unroll
  for (int i = 0; i < 3; ++i) {
    int e = t + 640 * i;
    if (i < 2 || e < DF_ * 4) z[i] = *(const float4*)(Zt + (size_t)(e >> 2) * S_ + s0 + 2 * (e & 3));
  }
  const int row = t / 80, c = t % 80;
  float xv[8];
  {
    uint4 a = *(const uint4*)(xin + (size_t)(s0 + row) * D_ + c * 8);
    unsigned u[4] = {a.x, a.y, a.z, a.w};
#pragma unroll
    for (int i = 0; i < 4; ++i) { xv[2 * i] = __uint_as_float(u[i] << 16); xv[2 * i + 1] = __uint_as_float(u[i] & 0xffff0000u); }
  }
  const int f = t / 160, j = t % 160;
  const float2 w2_ = cispi<1>((float)(j & 3) * (1.0f / 8.0f)), w3_ = cispi<1>((float)(j & 15) * (1.0f / 32.0f));
  const float2 w4_ = cispi<1>((float)j * (1.0f / 320.0f));
#pragma unroll
  for (int i = 0; i < 3; ++i) {
    int e = t + 640 * i;
    if (i < 2 || e < DF_ * 4) {
      int k = e >> 2, ff = e & 3;
      if (k == 0 || k == 320) {
        buf[0][ff][PD(k)] = make_float2(z[i].x, z[i].z);
      } else {
        buf[0][ff][PD(k)] = make_float2(z[i].x - z[i].w, z[i].y + z[i].z);
        buf[0][ff][PD(640 - k)] = make_float2(z[i].x + z[i].w, z[i].z - z[i].y);
      }
    }
  }
  __syncthreads();
  STAMP_AT();
  fft640<1>(buf[0][f], buf[1][f], j, w2_, w3_, w4_);
  __syncthreads();
  STAMP_AT();
  {
    const float* src = (const float*)&buf[0][row >> 1][0] + (row & 1);
    unsigned u[4];
#pragma unroll
    for (int i = 0; i < 8; ++i) xv[i] = rbf(xv[i] + rbf(src[2 * PD(c * 8 + i)]));
#pragma unroll
    for (int i = 0; i < 4; ++i) u[i] = (__float_as_uint(xv[2 * i]) >> 16) | (__float_as_uint(xv[2 * i + 1]) & 0xffff0000u);
    ST4U(xout + (size_t)(s0 + row) * D_ + c * 8, make_uint4(u[0], u[1], u[2], u[3]));
  }
  ln8(xv, red, row, c);
  {
    unsigned u[4];
#pragma unroll
    for (int i = 0; i < 4; ++i) u[i] = (__float_as_uint(xv[2 * i]) >> 16) | (__float_as_uint(xv[2 * i + 1]) & 0xffff0000u);
    ST4U(xln + kb_off(S_, s0 + row, c * 8), make_uint4(u[0], u[1], u[2], u[3]));
  }
  STAMP_AT();
  STAMP_OUT(4);
}

template <int N> __device__ __forceinline__ void wait_vm() { asm volatile("s_waitcnt vmcnt(%0)" ::"i"(N) : "memory"); }
__device__ __forceinline__ void wait_lgkm0() { asm volatile("s_waitcnt lgkmcnt(0)" ::: "memory"); }

constexpr int FP_ = 1792;

template <int NP, int L>
__device__ __forceinline__ void g1_body(const u16* __restrict__ A, const u16* __restrict__ Bt, u16* __restrict__ out,
                                        char* lds, int bm, int bn, int wm, int p0) {
  constexpr int BM = 128, BN = 224, NS = 6, NT = 20, PF = 5;
  constexpr int ABYTES = BM * 64, BBYTES = BN * 64, STG = ABYTES + BBYTES;
  const int t = threadIdx.x, lane = t & 63, fr = lane & 15, fq = lane >> 4;
  const char* Ag = (const char*)A + (size_t)bm * ABYTES + t * 16;
  const char* Bg = (const char*)Bt + (size_t)bn * BBYTES + t * 16;
  auto stage = [&](int ks) {
    char* la = lds + (ks % NS) * STG + t * 16;
    __builtin_amdgcn_global_load_lds((const unsigned*)(Ag + (size_t)ks * (S_ * 64)), (unsigned*)la, 16, 0, 0);
    __builtin_amdgcn_global_load_lds((const unsigned*)(Bg + (size_t)ks * (3584 * 64)), (unsigned*)(la + ABYTES), 16, 0, 0);
    if (L == 3)
      __builtin_amdgcn_global_load_lds((const unsigned*)(Bg + (size_t)ks * (3584 * 64) + 8192), (unsigned*)(la + ABYTES + 8192), 16, 0, 0);
  };
  const int aoff = (wm * 32 + fr) * 64 + ((fq ^ swz(fr)) << 4);
  const int boff = ABYTES + (p0 * 32 + fr) * 64 + ((fq ^ swz(fr)) << 4);
  bf16x8 af[2][2], bfr[2][2 * NP];
  auto ldfr = [&](int set, int ks) {
    const char* l = lds + (ks % NS) * STG;
#pragma unroll
    for (int m = 0; m < 2; ++m) af[set][m] = *(const bf16x8*)(l + aoff + m * 1024);
#pragma unroll
    for (int n = 0; n < 2 * NP; ++n) bfr[set][n] = *(const bf16x8*)(l + boff + n * 1024);
  };
  f32x4 acc[2][2 * NP];
#pragma unroll
  for (int m = 0; m < 2; ++m)
#pragma unroll
    for (int n = 0; n < 2 * NP; ++n) acc[m][n] = (f32x4){0.f, 0.f, 0.f, 0.f};
  STAMP_DECL STAMP_AT();
#pragma unroll
  for (int i = 0; i < PF; ++i) stage(i);
  wait_vm<(PF - 1) * L>();
  __builtin_amdgcn_s_barrier();
  ldfr(0, 0);
#pragma unroll
  for (int ks = 0; ks < NT; ++ks) {
    STAMP_AT();
    const int cur = ks & 1;
    if (ks + 1 < NT) {
      constexpr int dummy = 0; (void)dummy;
      const int issued = (ks + PF - 1 < NT - 1) ? ks + PF - 1 : NT - 1;
      const int allow = (issued - (ks + 1)) * L;
      if (allow >= 3 * L) wait_vm<3 * L>(); else if (allow == 2 * L) wait_vm<2 * L>(); else if (allow == L) wait_vm<L>(); else wait_vm<0>();
      __builtin_amdgcn_s_barrier();
      if (ks + PF < NT) stage(ks + PF);
      ldfr(cur ^ 1, ks + 1);
    }
#pragma unroll
    for (int n = 0; n < 2 * NP; ++n)
#pragma unroll
      for (int m = 0; m < 2; ++m)
        acc[m][n] = __builtin_amdgcn_mfma_f32_16x16x32_bf16(bfr[cur][n], af[cur][m], acc[m][n], 0, 0, 0);
    if (ks + 1 < NT) {
#pragma unroll
      for (int g = 0; g < L; ++g) {
        __builtin_amdgcn_sched_group_barrier(0x8, 1, 0);
        __builtin_amdgcn_sched_group_barrier(0x10, 1, 0);
      }
#pragma unroll
      for (int g = 0; g < 2 * NP + 2; ++g) {
        __builtin_amdgcn_sched_group_barrier(0x8, 1, 0);
        __builtin_amdgcn_sched_group_barrier(0x100, 1, 0);
      }
    }
    __builtin_amdgcn_sched_barrier(0);
  }
  STAMP_AT();
#pragma unroll
  for (int m = 0; m < 2; ++m) {
    int row = bm * BM + wm * 32 + m * 16 + fr;
#pragma unroll
    for (int p = 0; p < NP; ++p) {
      int hc = bn * 112 + (p0 + p) * 16 + fq * 4;
      u16 o[4];
#pragma unroll
      for (int j = 0; j < 4; ++j) {
        float g = rbf(acc[m][2 * p][j]), u = rbf(acc[m][2 * p + 1][j]);
        float sg = rbf(g * __builtin_amdgcn_rcpf(1.0f + __expf(-g)));
        o[j] = f2bf(sg * u);
      }
      ST2U(out + kb_off(S_, row, hc), make_uint2(o[0] | ((unsigned)o[1] << 16), o[2] | ((unsigned)o[3] << 16)));
    }
  }
  STAMP_AT();
  STAMP_OUT(0);
}
__global__ __launch_bounds__(512) void k_g1(const u16* __restrict__ A, const u16* __restrict__ Bt, u16* __restrict__ out) {
  __shared__ __attribute__((aligned(16))) char lds[6 * (128 + 224) * 64];
  const int wid = __builtin_amdgcn_readfirstlane(threadIdx.x >> 6);
  const int xcd = blockIdx.x & 7, idx = blockIdx.x >> 3;
  const int bm = (xcd >> 1) * 4 + (idx & 3), bn = (xcd & 1) * 8 + (idx >> 2);
  if (wid < 4) g1_body<4, 3>(A, Bt, out, lds, bm, bn, wid, 0);
  else if (wid < 6) g1_body<3, 3>(A, Bt, out, lds, bm, bn, wid - 4, 4);
  else g1_body<3, 2>(A, Bt, out, lds, bm, bn, wid - 4, 4);
}

template <int L>
__device__ __forceinline__ void g2_body(const u16* __restrict__ A, const u16* __restrict__ Bt, const u16* res, u16* out,
                                        char* lds, int bm, int bn, int wid) {
  constexpr int BM = 64, BN = 80, NS = 4, NT = 14, PF = 3;
  constexpr int ABYTES = 4 * BM * 64, BBYTES = 4 * BN * 64, STG = ABYTES + BBYTES;
  const int t = threadIdx.x, lane = t & 63, fr = lane & 15, fq = lane >> 4;
  const int ks4 = wid & 3, mh = wid >> 2;
  const char* Ag[2]; const char* Bg[3];
#pragma unroll
  for (int i = 0; i < 2; ++i) { int by = t * 16 + i * 8192; Ag[i] = (const char*)A + (size_t)(by >> 12) * (S_ * 64) + (size_t)bm * 4096 + (by & 4095); }
#pragma unroll
  for (int i = 0; i < 3; ++i) { int by = t * 16 + i * 8192; int kb = by / 5120; Bg[i] = (const char*)Bt + (size_t)kb * (D_ * 64) + (size_t)bn * 5120 + (by - kb * 5120); }
  auto stage = [&](int st) {
    char* la = lds + (st % NS) * STG + t * 16;
#pragma unroll
    for (int i = 0; i < 2; ++i)
      __builtin_amdgcn_global_load_lds((const unsigned*)(Ag[i] + (size_t)st * (4 * S_ * 64)), (unsigned*)(la + i * 8192), 16, 0, 0);
#pragma unroll
    for (int i = 0; i < L - 2; ++i)
      __builtin_amdgcn_global_load_lds((const unsigned*)(Bg[i] + (size_t)st * (4 * D_ * 64)), (unsigned*)(la + ABYTES + i * 8192), 16, 0, 0);
  };
  const int aoff = ks4 * 4096 + (mh * 32 + fr) * 64 + ((fq ^ swz(fr)) << 4);
  const int boff = ABYTES + ks4 * 5120 + fr * 64 + ((fq ^ swz(fr)) << 4);
  bf16x8 af[2][2], bfr[2][5];
  auto ldfr = [&](int set, int st) {
    const char* l = lds + (st % NS) * STG;
#pragma unroll
    for (int m = 0; m < 2; ++m) af[set][m] = *(const bf16x8*)(l + aoff + m * 1024);
#pragma unroll
    for (int n = 0; n < 5; ++n) bfr[set][n] = *(const bf16x8*)(l + boff + n * 1024);
  };
  f32x4 acc[2][5];
#pragma unroll
  for (int m = 0; m < 2; ++m)
#pragma unroll
    for (int n = 0; n < 5; ++n) acc[m][n] = (f32x4){0.f, 0.f, 0.f, 0.f};
  STAMP_DECL STAMP_AT();
  uint2 rvp[3];
#pragma unroll
  for (int i = 0; i < 3; ++i) {
    int fi = wid + 8 * i; if (fi >= 20) fi = wid;
    rvp[i] = *(const uint2*)(res + (size_t)(bm * BM + (fi / 5) * 16 + fr) * D_ + bn * BN + (fi % 5) * 16 + fq * 4);
  }
#pragma unroll
  for (int i = 0; i < PF; ++i) stage(i);
  wait_vm<(PF - 1) * L>();
  __builtin_amdgcn_s_barrier();
  ldfr(0, 0);
#pragma unroll
  for (int st = 0; st < NT; ++st) {
    STAMP_AT();
    const int cur = st & 1;
    if (st + 1 < NT) {
      const int issued = (st + PF - 1 < NT - 1) ? st + PF - 1 : NT - 1;
      const int allow = (issued - (st + 1)) * L;
      if (allow >= L) wait_vm<L>(); else wait_vm<0>();
      __builtin_amdgcn_s_barrier();
      if (st + PF < NT) stage(st + PF);
      ldfr(cur ^ 1, st + 1);
    }
#pragma unroll
    for (int m = 0; m < 2; ++m)
#pragma unroll
      for (int n = 0; n < 5; ++n)
        acc[m][n] = __builtin_amdgcn_mfma_f32_16x16x32_bf16(bfr[cur][n], af[cur][m], acc[m][n], 0, 0, 0);
    if (st + 1 < NT) {
#pragma unroll
      for (int g = 0; g < 5; ++g) {
        __builtin_amdgcn_sched_group_barrier(0x8, 1, 0);
        __builtin_amdgcn_sched_group_barrier(0x10, 1, 0);
        __builtin_amdgcn_sched_group_barrier(0x100, 1, 0);
      }
#pragma unroll
      for (int g = 0; g < 2; ++g) {
        __builtin_amdgcn_sched_group_barrier(0x8, 1, 0);
        __builtin_amdgcn_sched_group_barrier(0x100, 1, 0);
      }
    }
    __builtin_amdgcn_sched_barrier(0);
  }
  STAMP_AT();
  __syncthreads();
  f32x4* red = (f32x4*)lds;
#pragma unroll
  for (int m = 0; m < 2; ++m)
#pragma unroll
    for (int n = 0; n < 5; ++n) red[(ks4 * 20 + mh * 10 + m * 5 + n) * 64 + lane] = acc[m][n];
  __syncthreads();
#pragma unroll
  for (int i = 0; i < 3; ++i) {
    int fi = wid + 8 * i;
    if (fi < 20) {
      f32x4 s = red[fi * 64 + lane] + red[(20 + fi) * 64 + lane] + red[(40 + fi) * 64 + lane] + red[(60 + fi) * 64 + lane];
      int mrow = fi / 5, n = fi % 5;
      int row = bm * BM + mrow * 16 + fr;
      int cc = bn * BN + n * 16 + fq * 4;
      uint2 rv = rvp[i];
      float r0 = __uint_as_float(rv.x << 16), r1 = __uint_as_float(rv.x & 0xffff0000u);
      float r2 = __uint_as_float(rv.y << 16), r3 = __uint_as_float(rv.y & 0xffff0000u);
      u16 o0 = f2bf(r0 + rbf(s[0])), o1 = f2bf(r1 + rbf(s[1]));
      u16 o2 = f2bf(r2 + rbf(s[2])), o3 = f2bf(r3 + rbf(s[3]));
      ST2U(out + (size_t)row * D_ + cc, make_uint2(o0 | ((unsigned)o1 << 16), o2 | ((unsigned)o3 << 16)));
    }
  }
  STAMP_AT();
  STAMP_OUT(1);
}
__global__ __launch_bounds__(512) void k_g2(const u16* __restrict__ A, const u16* __restrict__ Bt, const u16* res, u16* out) {
  __shared__ __attribute__((aligned(16))) char lds[4 * 4 * (64 + 80) * 64];
  const int wid = __builtin_amdgcn_readfirstlane(threadIdx.x >> 6);
  const int xcd = blockIdx.x & 7, idx = blockIdx.x >> 3;
  const int bm = xcd * 4 + (idx & 3), bn = idx >> 2;
  if (wid < 4) g2_body<5>(A, Bt, res, out, lds, bm, bn, wid);
  else g2_body<4>(A, Bt, res, out, lds, bm, bn, wid);
}

extern "C" void kernel_launch(void* const* d_in, const int* in_sizes, int n_in,
                              void* d_out, int out_size, void* d_ws, size_t ws_size,
                              hipStream_t stream) {
  const u16* Ax = (const u16*)d_in[0];
  const u16* wr = (const u16*)d_in[1];
  const u16* wi = (const u16*)d_in[2];
  const u16* w1 = (const u16*)d_in[4];
  const u16* w3 = (const u16*)d_in[5];
  const u16* w2 = (const u16*)d_in[6];
  char* ws = (char*)d_ws;
  size_t off = 0;
  auto alloc = [&](size_t bytes) { void* p = ws + off; off += (bytes + 255) & ~(size_t)255; return p; };
  float2* Zt = (float2*)alloc((size_t)DF_ * S_ * 8);
  unsigned* Wt = (unsigned*)alloc((size_t)DF_ * S_ * 4);
  u16* xb = (u16*)alloc((size_t)S_ * D_ * 2);
  u16* xln = (u16*)alloc((size_t)S_ * D_ * 2);
  u16* h = (u16*)alloc((size_t)S_ * 1792 * 2);
  u16* w13p = (u16*)alloc((size_t)16 * 224 * D_ * 2);
  u16* w2t = (u16*)alloc((size_t)D_ * 1792 * 2);
  u16* outp = (u16*)d_out;

  for (int layer = 0; layer < 2; ++layer) {
    const u16* xin = layer == 0 ? Ax : xb;
    k_a1<<<layer == 0 ? 384 + 160 : 256, 640, 0, stream>>>(xin, Zt, wr, wi, Wt, w1, w3, w2, w13p, w2t);
    k_a2<<<layer == 0 ? DF_ + 160 : DF_, 256, 0, stream>>>(Zt, Wt, w1, w3, w2, w13p, w2t);
    k_a3<<<layer == 0 ? 256 + 160 : 256, 640, 0, stream>>>(Zt, xin, xb, xln, w1, w3, w2, w13p, w2t);
    k_g1<<<256, 512, 0, stream>>>(xln, w13p, h);
    k_g2<<<256, 512, 0, stream>>>(h, w2t, xb, layer == 0 ? xb : outp);
  }
}
```

```cpp
#include <hip/hip_runtime.h>
#include <hip/hip_bf16.h>
#include <stdint.h>

typedef unsigned short u16;
using bf16x8 = __attribute__((ext_vector_type(8))) short;
using f32x4 = __attribute__((ext_vector_type(4))) float;

constexpr int S_ = 2048, D_ = 640, DF_ = 321, F_ = 1728;

__device__ __forceinline__ float bf2f(u16 v) { return __uint_as_float(((unsigned)v) << 16); }
__device__ __forceinline__ u16 f2bf(float f) { return __bfloat16_as_ushort(__float2bfloat16(f)); }
__device__ __forceinline__ float rbf(float f) { return bf2f(f2bf(f)); }

__device__ __forceinline__ float2 cadd(float2 a, float2 b) { return make_float2(a.x + b.x, a.y + b.y); }
__device__ __forceinline__ float2 csub(float2 a, float2 b) { return make_float2(a.x - b.x, a.y - b.y); }
__device__ __forceinline__ float2 cmul(float2 a, float2 b) {
  return make_float2(a.x * b.x - a.y * b.y, a.x * b.y + a.y * b.x);
}
template <int SG> __device__ __forceinline__ float2 mul_i(float2 a) {
  return SG > 0 ? make_float2(-a.y, a.x) : make_float2(a.y, -a.x);
}
template <int SG> __device__ __forceinline__ float2 twc(float2 w) {
  return SG > 0 ? make_float2(w.x, -w.y) : w;
}

template <int SG> __device__ __forceinline__ void dft4(float2& a0, float2& a1, float2& a2, float2& a3) {
  float2 s0 = cadd(a0, a2), d0 = csub(a0, a2), s1 = cadd(a1, a3), d1 = mul_i<SG>(csub(a1, a3));
  a0 = cadd(s0, s1); a2 = csub(s0, s1); a1 = cadd(d0, d1); a3 = csub(d0, d1);
}
template <int SG> __device__ __forceinline__ void dft8(float2* v) {
  float2 e0 = v[0], e1 = v[2], e2 = v[4], e3 = v[6], o0 = v[1], o1 = v[3], o2 = v[5], o3 = v[7];
  dft4<SG>(e0, e1, e2, e3); dft4<SG>(o0, o1, o2, o3);
  const float h = 0.70710678118654752440f;
  float2 t1 = make_float2(h * (o1.x - SG * o1.y), h * (o1.y + SG * o1.x));
  float2 t2 = mul_i<SG>(o2);
  float2 t3 = make_float2(h * (-o3.x - SG * o3.y), h * (-o3.y + SG * o3.x));
  v[0] = cadd(e0, o0); v[4] = csub(e0, o0);
  v[1] = cadd(e1, t1); v[5] = csub(e1, t1);
  v[2] = cadd(e2, t2); v[6] = csub(e2, t2);
  v[3] = cadd(e3, t3); v[7] = csub(e3, t3);
}
template <int SG> __device__ __forceinline__ void dft5(float2& a0, float2& a1, float2& a2, float2& a3, float2& a4) {
  const float c1 = 0.30901699437494742410f, c2 = -0.80901699437494742410f;
  const float s1 = 0.95105651629515357212f, s2 = 0.58778525229247312917f;
  float2 p1 = cadd(a1, a4), m1 = csub(a1, a4), p2 = cadd(a2, a3), m2 = csub(a2, a3);
  float2 t1 = make_float2(a0.x + c1 * p1.x + c2 * p2.x, a0.y + c1 * p1.y + c2 * p2.y);
  float2 t2 = make_float2(a0.x + c2 * p1.x + c1 * p2.x, a0.y + c2 * p1.y + c1 * p2.y);
  float2 u1 = mul_i<SG>(make_float2(s1 * m1.x + s2 * m2.x, s1 * m1.y + s2 * m2.y));
  float2 u2 = mul_i<SG>(make_float2(s2 * m1.x - s1 * m2.x, s2 * m1.y - s1 * m2.y));
  a0 = cadd(a0, cadd(p1, p2));
  a1 = cadd(t1, u1); a4 = csub(t1, u1);
  a2 = cadd(t2, u2); a3 = csub(t2, u2);
}
template <int SG> __device__ __forceinline__ void dft10(float2* v) {
  float2 e0 = v[0], e1 = v[2], e2 = v[4], e3 = v[6], e4 = v[8];
  float2 o0 = v[1], o1 = v[3], o2 = v[5], o3 = v[7], o4 = v[9];
  dft5<SG>(e0, e1, e2, e3, e4); dft5<SG>(o0, o1, o2, o3, o4);
  const float c1 = 0.80901699437494742410f, s1 = 0.58778525229247312917f;
  const float c2 = 0.30901699437494742410f, s2 = 0.95105651629515357212f;
  o1 = cmul(o1, make_float2(c1, SG * s1));
  o2 = cmul(o2, make_float2(c2, SG * s2));
  o3 = cmul(o3, make_float2(-c2, SG * s2));
  o4 = cmul(o4, make_float2(-c1, SG * s1));
  v[0] = cadd(e0, o0); v[5] = csub(e0, o0);
  v[1] = cadd(e1, o1); v[6] = csub(e1, o1);
  v[2] = cadd(e2, o2); v[7] = csub(e2, o2);
  v[3] = cadd(e3, o3); v[8] = csub(e3, o3);
  v[4] = cadd(e4, o4); v[9] = csub(e4, o4);
}

__device__ __forceinline__ int PD(int i) { return i + (i >> 3); }
#define ST4F(p, v) (*(float4*)(p) = (v))
#define ST2F(p, v) (*(float2*)(p) = (v))
#define ST4U(p, v) (*(uint4*)(p) = (v))
#define ST2U(p, v) (*(uint2*)(p) = (v))
__device__ __forceinline__ int swz(int row) { return (-(row >> 2)) & 3; }
__device__ __forceinline__ size_t kb_off(int R, int row, int k) {
  return (size_t)(k >> 5) * ((size_t)R * 32) + (size_t)row * 32 + ((((k >> 3) & 3) ^ swz(row)) << 3) + (k & 7);
}

template <int SG> __device__ __forceinline__ float2 cispi(float x) {
  float sn, cs; sincospif(x, &sn, &cs);
  return make_float2(cs, SG * sn);
}
template <int R> __device__ __forceinline__ void twiddle_pow(float2* v, float2 w1) {
  float2 w2 = cmul(w1, w1), w3 = cmul(w2, w1), w4 = cmul(w2, w2);
  v[1] = cmul(v[1], w1); v[2] = cmul(v[2], w2); v[3] = cmul(v[3], w3);
  if (R > 4) {
    float2 w5 = cmul(w4, w1), w6 = cmul(w3, w3), w7 = cmul(w4, w3);
    v[4] = cmul(v[4], w4); v[5] = cmul(v[5], w5); v[6] = cmul(v[6], w6); v[7] = cmul(v[7], w7);
    if (R > 8) { float2 w8 = cmul(w4, w4), w9 = cmul(w8, w1); v[8] = cmul(v[8], w8); v[9] = cmul(v[9], w9); }
  }
}

template <int SG>
__device__ __forceinline__ void fft640(float2* b0, float2* b1, int j, float2 w2_, float2 w3_, float2 w4_) {
  float2 v[10];
#pragma unroll
  for (int r = 0; r < 4; ++r) v[r] = b0[PD(j + 160 * r)];
  dft4<SG>(v[0], v[1], v[2], v[3]);
#pragma unroll
  for (int r = 0; r < 4; ++r) b1[PD(4 * j + r)] = v[r];
  __syncthreads();
#pragma unroll
  for (int r = 0; r < 4; ++r) v[r] = b1[PD(j + 160 * r)];
  twiddle_pow<4>(v, w2_);
  dft4<SG>(v[0], v[1], v[2], v[3]);
#pragma unroll
  for (int r = 0; r < 4; ++r) b0[PD((j >> 2) * 16 + (j & 3) + 4 * r)] = v[r];
  __syncthreads();
#pragma unroll
  for (int r = 0; r < 4; ++r) v[r] = b0[PD(j + 160 * r)];
  twiddle_pow<4>(v, w3_);
  dft4<SG>(v[0], v[1], v[2], v[3]);
#pragma unroll
  for (int r = 0; r < 4; ++r) b1[PD((j >> 4) * 64 + (j & 15) + 16 * r)] = v[r];
  __syncthreads();
  if (j < 64) {
#pragma unroll
    for (int r = 0; r < 10; ++r) v[r] = b1[PD(j + 64 * r)];
    twiddle_pow<10>(v, w4_);
    dft10<SG>(v);
#pragma unroll
    for (int r = 0; r < 10; ++r) b0[PD(j + 64 * r)] = v[r];
  }
}

__device__ __forceinline__ void ln8(float* xv, float* red, int row, int c) {
  float s = 0.f;
#pragma unroll
  for (int i = 0; i < 8; ++i) s += xv[i];
  s += __shfl_xor(s, 1); s += __shfl_xor(s, 2); s += __shfl_xor(s, 4);
  if ((c & 7) == 0) red[row * 10 + (c >> 3)] = s;
  __syncthreads();
  float mean = 0.f;
#pragma unroll
  for (int i = 0; i < 10; ++i) mean += red[row * 10 + i];
  mean *= (1.0f / 640.0f);
  float q = 0.f;
#pragma unroll
  for (int i = 0; i < 8; ++i) { float d = xv[i] - mean; q += d * d; }
  q += __shfl_xor(q, 1); q += __shfl_xor(q, 2); q += __shfl_xor(q, 4);
  if ((c & 7) == 0) red[80 + row * 10 + (c >> 3)] = q;
  __syncthreads();
  float var = 0.f;
#pragma unroll
  for (int i = 0; i < 10; ++i) var += red[80 + row * 10 + i];
  var *= (1.0f / 640.0f);
  float mb = rbf(mean), vb = rbf(var);
  float den = rbf(sqrtf(rbf(vb + 1e-5f)));
  float inv = 1.0f / den;
#pragma unroll
  for (int i = 0; i < 8; ++i) xv[i] = rbf(rbf(xv[i] - mb) * inv);
}

constexpr int FB = 720;

__device__ __forceinline__ void pack_w13(const u16* __restrict__ src, u16* __restrict__ dst, int which, int nb, int k0, u16* sm) {
  const int t = threadIdx.x;
  for (int e = t; e < 64 * 27; e += blockDim.x) {
    int r = e / 27, q = e % 27;
    uint2 v = *(const uint2*)(src + (size_t)(k0 + r) * F_ + nb * 108 + q * 4);
    sm[(q * 4 + 0) * 66 + r] = (u16)(v.x & 0xffff); sm[(q * 4 + 1) * 66 + r] = (u16)(v.x >> 16);
    sm[(q * 4 + 2) * 66 + r] = (u16)(v.y & 0xffff); sm[(q * 4 + 3) * 66 + r] = (u16)(v.y >> 16);
  }
  __syncthreads();
  for (int e = t; e < 112 * 8; e += blockDim.x) {
    int c = e >> 3, q = e & 7;
    uint4 v = make_uint4(0, 0, 0, 0);
    if (c < 108) {
      u16* pp = (u16*)&v;
#pragma unroll
      for (int i = 0; i < 8; ++i) pp[i] = sm[c * 66 + q * 8 + i];
    }
    int drow = nb * 224 + (c >> 4) * 32 + which * 16 + (c & 15);
    *(uint4*)(dst + kb_off(16 * 224, drow, k0 + q * 8)) = v;
  }
}
__device__ __forceinline__ void pack_w2(const u16* __restrict__ src, u16* __restrict__ dst, int nb, int d0, u16* sm) {
  const int t = threadIdx.x;
  for (int e = t; e < 108 * 8; e += blockDim.x) {
    int i = e >> 3, q = e & 7;
    uint4 v = *(const uint4*)(src + (size_t)(nb * 108 + i) * D_ + d0 + q * 8);
    u16* pp = (u16*)&v;
#pragma unroll
    for (int j = 0; j < 8; ++j) sm[(q * 8 + j) * 114 + i] = pp[j];
  }
  __syncthreads();
  for (int e = t; e < 64 * 14; e += blockDim.x) {
    int d = e / 14, q = e % 14;
    uint4 v; u16* pp = (u16*)&v;
#pragma unroll
    for (int j = 0; j < 8; ++j) { int i = q * 8 + j; pp[j] = i < 108 ? sm[d * 114 + i] : (u16)0; }
    *(uint4*)(dst + kb_off(D_, d0 + d, nb * 112 + q * 8)) = v;
  }
}

__device__ __forceinline__ void pack_job(int id, const u16* w1, const u16* w3, const u16* w2, u16* w13p, u16* w2t, u16* sm) {
  if (id < 320) {
    int which = id / 160, r = id % 160;
    pack_w13(which ? w3 : w1, w13p, which, r / 10, (r % 10) * 64, sm);
  } else {
    int r = id - 320;
    pack_w2(w2, w2t, r / 10, (r % 10) * 64, sm);
  }
}

__global__ __launch_bounds__(640) void k_a1(const u16* __restrict__ x, float2* __restrict__ Zt,
                                            const u16* __restrict__ wr, const u16* __restrict__ wi,
                                            unsigned* __restrict__ Wt,
                                            const u16* __restrict__ w1, const u16* __restrict__ w3, const u16* __restrict__ w2,
                                            u16* __restrict__ w13p, u16* __restrict__ w2t) {
  __shared__ float2 buf[2][4][FB];
  __shared__ float red[160];
  const int t = threadIdx.x;
  if (blockIdx.x >= 384) { pack_job(blockIdx.x - 384, w1, w3, w2, w13p, w2t, (u16*)&buf[0][0][0]); return; }
  if (blockIdx.x >= 256) {
    const int s0 = (blockIdx.x - 256) * 16 + (t >= 320 ? 8 : 0);
    for (int k = t % 320; k < DF_; k += 320) {
      unsigned p[8];
#pragma unroll
      for (int r = 0; r < 8; ++r)
        p[r] = (unsigned)wr[(s0 + r) * DF_ + k] | ((unsigned)wi[(s0 + r) * DF_ + k] << 16);
      uint4* dst = (uint4*)(Wt + (size_t)k * S_ + s0);
      dst[0] = make_uint4(p[0], p[1], p[2], p[3]);
      dst[1] = make_uint4(p[4], p[5], p[6], p[7]);
    }
    return;
  }
  const int s0 = blockIdx.x * 8;
  const int row = t / 80, c = t % 80;
  float xv[8];
  {
    uint4 a = *(const uint4*)(x + (size_t)(s0 + row) * D_ + c * 8);
    unsigned u[4] = {a.x, a.y, a.z, a.w};
#pragma unroll
    for (int i = 0; i < 4; ++i) { xv[2 * i] = __uint_as_float(u[i] << 16); xv[2 * i + 1] = __uint_as_float(u[i] & 0xffff0000u); }
  }
  const int f = t / 160, j = t % 160;
  const float2 w2_ = cispi<-1>((float)(j & 3) * (1.0f / 8.0f)), w3_ = cispi<-1>((float)(j & 15) * (1.0f / 32.0f));
  const float2 w4_ = cispi<-1>((float)j * (1.0f / 320.0f));
  ln8(xv, red, row, c);
  {
    float* dst = (float*)&buf[0][row >> 1][0] + (row & 1);
#pragma unroll
    for (int i = 0; i < 8; ++i) dst[2 * PD(c * 8 + i)] = xv[i];
  }
  __syncthreads();
  fft640<-1>(buf[0][f], buf[1][f], j, w2_, w3_, w4_);
  __syncthreads();
#pragma unroll
  for (int i = 0; i < 3; ++i) {
    int e = t + 640 * i;
    if (i < 2 || e < DF_ * 4) {
      int k = e >> 2, ff = e & 3;
      float2 a = buf[0][ff][PD(k)], b = buf[0][ff][PD(k == 0 ? 0 : 640 - k)];
      float4 o;
      o.x = 0.5f * (a.x + b.x); o.y = 0.5f * (a.y - b.y);
      o.z = 0.5f * (a.y + b.y); o.w = 0.5f * (b.x - a.x);
      ST4F(Zt + (size_t)k * S_ + s0 + 2 * ff, o);
    }
  }
}

constexpr int CB = 2048 + 256;
__global__ __launch_bounds__(256) void k_a2(float2* __restrict__ Zt, const unsigned* __restrict__ Wt,
                                            const u16* __restrict__ w1, const u16* __restrict__ w3, const u16* __restrict__ w2,
                                            u16* __restrict__ w13p, u16* __restrict__ w2t) {
  __shared__ float2 smem[2 * CB];
  const int t = threadIdx.x;
  if (blockIdx.x >= DF_) {
    pack_job(blockIdx.x - DF_ + 160, w1, w3, w2, w13p, w2t, (u16*)smem);
    return;
  }
  float2* b0 = smem; float2* b1 = smem + CB;
  const int k = blockIdx.x;
  float2* col = Zt + (size_t)k * S_;
  float2 v[8];
#pragma unroll
  for (int r = 0; r < 8; ++r) v[r] = col[t + 256 * r];
  unsigned wv[8];
#pragma unroll
  for (int r = 0; r < 8; ++r) wv[r] = Wt[(size_t)k * S_ + t + 256 * r];
  const float2 wp2 = cispi<-1>((float)(t & 7) * (1.0f / 32.0f));
  const float2 wp3 = cispi<-1>((float)(t & 63) * (1.0f / 256.0f));
  const float2 wp4a = cispi<-1>((float)t * (1.0f / 1024.0f)), wp4b = cispi<-1>((float)(t + 256) * (1.0f / 1024.0f));
  dft8<-1>(v);
#pragma unroll
  for (int r = 0; r < 8; ++r) b0[PD(8 * t + r)] = v[r];
  __syncthreads();
  {
    int m = t & 7;
#pragma unroll
    for (int r = 0; r < 8; ++r) v[r] = b0[PD(t + 256 * r)];
    twiddle_pow<8>(v, wp2);
    dft8<-1>(v);
#pragma unroll
    for (int r = 0; r < 8; ++r) b1[PD((t >> 3) * 64 + m + 8 * r)] = v[r];
  }
  __syncthreads();
  {
    int m = t & 63;
#pragma unroll
    for (int r = 0; r < 8; ++r) v[r] = b1[PD(t + 256 * r)];
    twiddle_pow<8>(v, wp3);
    dft8<-1>(v);
#pragma unroll
    for (int r = 0; r < 8; ++r) b0[PD((t >> 6) * 512 + m + 64 * r)] = v[r];
  }
  __syncthreads();
#pragma unroll
  for (int h = 0; h < 2; ++h) {
    int j = t + 256 * h;
    float2 a[4] = {b0[PD(j)], b0[PD(j + 512)], b0[PD(j + 1024)], b0[PD(j + 1536)]};
    twiddle_pow<4>(a, h ? wp4b : wp4a);
    dft4<-1>(a[0], a[1], a[2], a[3]);
    v[h] = a[0]; v[h + 2] = a[1]; v[h + 4] = a[2]; v[h + 6] = a[3];
  }
  const float sc = 1.0f / (2048.0f * 640.0f);
#pragma unroll
  for (int r = 0; r < 8; ++r) {
    float2 w = make_float2(__uint_as_float(wv[r] << 16) * sc, __uint_as_float(wv[r] & 0xffff0000u) * sc);
    v[r] = cmul(v[r], w);
  }
  dft8<1>(v);
#pragma unroll
  for (int r = 0; r < 8; ++r) b1[PD(8 * t + r)] = v[r];
  __syncthreads();
  {
    int m = t & 7;
#pragma unroll
    for (int r = 0; r < 8; ++r) v[r] = b1[PD(t + 256 * r)];
    twiddle_pow<8>(v, twc<1>(wp2));
    dft8<1>(v);
#pragma unroll
    for (int r = 0; r < 8; ++r) b0[PD((t >> 3) * 64 + m + 8 * r)] = v[r];
  }
  __syncthreads();
  {
    int m = t & 63;
#pragma unroll
    for (int r = 0; r < 8; ++r) v[r] = b0[PD(t + 256 * r)];
    twiddle_pow<8>(v, twc<1>(wp3));
    dft8<1>(v);
#pragma unroll
    for (int r = 0; r < 8; ++r) b1[PD((t >> 6) * 512 + m + 64 * r)] = v[r];
  }
  __syncthreads();
#pragma unroll
  for (int h = 0; h < 2; ++h) {
    int j = t + 256 * h;
    float2 a[4] = {b1[PD(j)], b1[PD(j + 512)], b1[PD(j + 1024)], b1[PD(j + 1536)]};
    twiddle_pow<4>(a, twc<1>(h ? wp4b : wp4a));
    dft4<1>(a[0], a[1], a[2], a[3]);
    ST2F(col + j, a[0]); ST2F(col + j + 512, a[1]); ST2F(col + j + 1024, a[2]); ST2F(col + j + 1536, a[3]);
  }
}

__global__ __launch_bounds__(640) void k_a3(const float2* __restrict__ Zt, const u16* xin,
                                            u16* xout, u16* __restrict__ xln,
                                            const u16* __restrict__ w1, const u16* __restrict__ w3, const u16* __restrict__ w2,
                                            u16* __restrict__ w13p, u16* __restrict__ w2t) {
  __shared__ float2 buf[2][4][FB];
  __shared__ float red[160];
  if (blockIdx.x >= 256) { pack_job(blockIdx.x - 256 + 320, w1, w3, w2, w13p, w2t, (u16*)&buf[0][0][0]); return; }
  const int t = threadIdx.x, s0 = blockIdx.x * 8;
  float4 z[3];
#pragma unroll
  for (int i = 0; i < 3; ++i) {
    int e = t + 640 * i;
    if (i < 2 || e < DF_ * 4) z[i] = *(const float4*)(Zt + (size_t)(e >> 2) * S_ + s0 + 2 * (e & 3));
  }
  const int row = t / 80, c = t % 80;
  float xv[8];
  {
    uint4 a = *(const uint4*)(xin + (size_t)(s0 + row) * D_ + c * 8);
    unsigned u[4] = {a.x, a.y, a.z, a.w};
#pragma unroll
    for (int i = 0; i < 4; ++i) { xv[2 * i] = __uint_as_float(u[i] << 16); xv[2 * i + 1] = __uint_as_float(u[i] & 0xffff0000u); }
  }
  const int f = t / 160, j = t % 160;
  const float2 w2_ = cispi<1>((float)(j & 3) * (1.0f / 8.0f)), w3_ = cispi<1>((float)(j & 15) * (1.0f / 32.0f));
  const float2 w4_ = cispi<1>((float)j * (1.0f / 320.0f));
#pragma unroll
  for (int i = 0; i < 3; ++i) {
    int e = t + 640 * i;
    if (i < 2 || e < DF_ * 4) {
      int k = e >> 2, ff = e & 3;
      if (k == 0 || k == 320) {
        buf[0][ff][PD(k)] = make_float2(z[i].x, z[i].z);
      } else {
        buf[0][ff][PD(k)] = make_float2(z[i].x - z[i].w, z[i].y + z[i].z);
        buf[0][ff][PD(640 - k)] = make_float2(z[i].x + z[i].w, z[i].z - z[i].y);
      }
    }
  }
  __syncthreads();
  fft640<1>(buf[0][f], buf[1][f], j, w2_, w3_, w4_);
  __syncthreads();
  {
    const float* src = (const float*)&buf[0][row >> 1][0] + (row & 1);
    unsigned u[4];
#pragma unroll
    for (int i = 0; i < 8; ++i) xv[i] = rbf(xv[i] + rbf(src[2 * PD(c * 8 + i)]));
#pragma unroll
    for (int i = 0; i < 4; ++i) u[i] = (__float_as_uint(xv[2 * i]) >> 16) | (__float_as_uint(xv[2 * i + 1]) & 0xffff0000u);
    ST4U(xout + (size_t)(s0 + row) * D_ + c * 8, make_uint4(u[0], u[1], u[2], u[3]));
  }
  ln8(xv, red, row, c);
  {
    unsigned u[4];
#pragma unroll
    for (int i = 0; i < 4; ++i) u[i] = (__float_as_uint(xv[2 * i]) >> 16) | (__float_as_uint(xv[2 * i + 1]) & 0xffff0000u);
    ST4U(xln + kb_off(S_, s0 + row, c * 8), make_uint4(u[0], u[1], u[2], u[3]));
  }
}

template <int N> __device__ __forceinline__ void wait_vm() { asm volatile("s_waitcnt vmcnt(%0)" ::"i"(N) : "memory"); }
__device__ __forceinline__ void wait_lgkm0() { asm volatile("s_waitcnt lgkmcnt(0)" ::: "memory"); }

constexpr int FP_ = 1792;

template <int NP, int L>
__device__ __forceinline__ void g1_body(const u16* __restrict__ A, const u16* __restrict__ Bt, u16* __restrict__ out,
                                        char* lds, int bm, int bn, int wm, int p0) {
  constexpr int BM = 128, BN = 224, NS = 6, NT = 20, PF = 5;
  constexpr int ABYTES = BM * 64, BBYTES = BN * 64, STG = ABYTES + BBYTES;
  const int t = threadIdx.x, lane = t & 63, fr = lane & 15, fq = lane >> 4;
  const char* Ag = (const char*)A + (size_t)bm * ABYTES + t * 16;
  const char* Bg = (const char*)Bt + (size_t)bn * BBYTES + t * 16;
  auto stage = [&](int ks) {
    char* la = lds + (ks % NS) * STG + t * 16;
    __builtin_amdgcn_global_load_lds((const unsigned*)(Ag + (size_t)ks * (S_ * 64)), (unsigned*)la, 16, 0, 0);
    __builtin_amdgcn_global_load_lds((const unsigned*)(Bg + (size_t)ks * (3584 * 64)), (unsigned*)(la + ABYTES), 16, 0, 0);
    if (L == 3)
      __builtin_amdgcn_global_load_lds((const unsigned*)(Bg + (size_t)ks * (3584 * 64) + 8192), (unsigned*)(la + ABYTES + 8192), 16, 0, 0);
  };
  const int aoff = (wm * 32 + fr) * 64 + ((fq ^ swz(fr)) << 4);
  const int boff = ABYTES + (p0 * 32 + fr) * 64 + ((fq ^ swz(fr)) << 4);
  bf16x8 af[2][2], bfr[2][2 * NP];
  auto ldfr = [&](int set, int ks) {
    const char* l = lds + (ks % NS) * STG;
#pragma unroll
    for (int m = 0; m < 2; ++m) af[set][m] = *(const bf16x8*)(l + aoff + m * 1024);
#pragma unroll
    for (int n = 0; n < 2 * NP; ++n) bfr[set][n] = *(const bf16x8*)(l + boff + n * 1024);
  };
  f32x4 acc[2][2 * NP];
#pragma unroll
  for (int m = 0; m < 2; ++m)
#pragma unroll
    for (int n = 0; n < 2 * NP; ++n) acc[m][n] = (f32x4){0.f, 0.f, 0.f, 0.f};
#pragma unroll
  for (int i = 0; i < PF; ++i) stage(i);
  wait_vm<(PF - 1) * L>();
  __builtin_amdgcn_s_barrier();
  ldfr(0, 0);
#pragma unroll
  for (int ks = 0; ks < NT; ++ks) {
    const int cur = ks & 1;
    if (ks + 1 < NT) {
      const int issued = (ks + PF - 1 < NT - 1) ? ks + PF - 1 : NT - 1;
      const int allow = (issued - (ks + 1)) * L;
      if (allow >= 3 * L) wait_vm<3 * L>(); else if (allow == 2 * L) wait_vm<2 * L>(); else if (allow == L) wait_vm<L>(); else wait_vm<0>();
      __builtin_amdgcn_s_barrier();
      if (ks + PF < NT) stage(ks + PF);
      ldfr(cur ^ 1, ks + 1);
    }
#pragma unroll
    for (int n = 0; n < 2 * NP; ++n)
#pragma unroll
      for (int m = 0; m < 2; ++m)
        acc[m][n] = __builtin_amdgcn_mfma_f32_16x16x32_bf16(bfr[cur][n], af[cur][m], acc[m][n], 0, 0, 0);
    if (ks + 1 < NT) {
#pragma unroll
      for (int g = 0; g < L; ++g) {
        __builtin_amdgcn_sched_group_barrier(0x8, 1, 0);
        __builtin_amdgcn_sched_group_barrier(0x10, 1, 0);
      }
#pragma unroll
      for (int g = 0; g < 2 * NP + 2; ++g) {
        __builtin_amdgcn_sched_group_barrier(0x8, 1, 0);
        __builtin_amdgcn_sched_group_barrier(0x100, 1, 0);
      }
    }
    __builtin_amdgcn_sched_barrier(0);
  }
#pragma unroll
  for (int m = 0; m < 2; ++m) {
    int row = bm * BM + wm * 32 + m * 16 + fr;
#pragma unroll
    for (int p = 0; p < NP; ++p) {
      int hc = bn * 112 + (p0 + p) * 16 + fq * 4;
      u16 o[4];
#pragma unroll
      for (int j = 0; j < 4; ++j) {
        float g = rbf(acc[m][2 * p][j]), u = rbf(acc[m][2 * p + 1][j]);
        float sg = rbf(g * __builtin_amdgcn_rcpf(1.0f + __expf(-g)));
        o[j] = f2bf(sg * u);
      }
      ST2U(out + kb_off(S_, row, hc), make_uint2(o[0] | ((unsigned)o[1] << 16), o[2] | ((unsigned)o[3] << 16)));
    }
  }
}
__global__ __launch_bounds__(512) void k_g1(const u16* __restrict__ A, const u16* __restrict__ Bt, u16* __restrict__ out) {
  __shared__ __attribute__((aligned(16))) char lds[6 * (128 + 224) * 64];
  const int wid = __builtin_amdgcn_readfirstlane(threadIdx.x >> 6);
  const int xcd = blockIdx.x & 7, idx = blockIdx.x >> 3;
  const int bm = (xcd >> 1) * 4 + (idx & 3), bn = (xcd & 1) * 8 + (idx >> 2);
  if (wid < 4) g1_body<4, 3>(A, Bt, out, lds, bm, bn, wid, 0);
  else if (wid < 6) g1_body<3, 3>(A, Bt, out, lds, bm, bn, wid - 4, 4);
  else g1_body<3, 2>(A, Bt, out, lds, bm, bn, wid - 4, 4);
}

template <int L>
__device__ __forceinline__ void g2_body(const u16* __restrict__ A, const u16* __restrict__ Bt, const u16* res, u16* out,
                                        char* lds, int bm, int bn, int wid) {
  constexpr int BM = 64, BN = 80, NS = 4, NT = 14, PF = 3;
  constexpr int ABYTES = 4 * BM * 64, BBYTES = 4 * BN * 64, STG = ABYTES + BBYTES;
  const int t = threadIdx.x, lane = t & 63, fr = lane & 15, fq = lane >> 4;
  const int ks4 = wid & 3, mh = wid >> 2;
  const char* Ag[2]; const char* Bg[3];
#pragma unroll
  for (int i = 0; i < 2; ++i) { int by = t * 16 + i * 8192; Ag[i] = (const char*)A + (size_t)(by >> 12) * (S_ * 64) + (size_t)bm * 4096 + (by & 4095); }
#pragma unroll
  for (int i = 0; i < 3; ++i) { int by = t * 16 + i * 8192; int kb = by / 5120; Bg[i] = (const char*)Bt + (size_t)kb * (D_ * 64) + (size_t)bn * 5120 + (by - kb * 5120); }
  auto stage = [&](int st) {
    char* la = lds + (st % NS) * STG + t * 16;
#pragma unroll
    for (int i = 0; i < 2; ++i)
      __builtin_amdgcn_global_load_lds((const unsigned*)(Ag[i] + (size_t)st * (4 * S_ * 64)), (unsigned*)(la + i * 8192), 16, 0, 0);
#pragma unroll
    for (int i = 0; i < L - 2; ++i)
      __builtin_amdgcn_global_load_lds((const unsigned*)(Bg[i] + (size_t)st * (4 * D_ * 64)), (unsigned*)(la + ABYTES + i * 8192), 16, 0, 0);
  };
  const int aoff = ks4 * 4096 + (mh * 32 + fr) * 64 + ((fq ^ swz(fr)) << 4);
  const int boff = ABYTES + ks4 * 5120 + fr * 64 + ((fq ^ swz(fr)) << 4);
  bf16x8 af[2][2], bfr[2][5];
  auto ldfr = [&](int set, int st) {
    const char* l = lds + (st % NS) * STG;
#pragma unroll
    for (int m = 0; m < 2; ++m) af[set][m] = *(const bf16x8*)(l + aoff + m * 1024);
#pragma unroll
    for (int n = 0; n < 5; ++n) bfr[set][n] = *(const bf16x8*)(l + boff + n * 1024);
  };
  f32x4 acc[2][5];
#pragma unroll
  for (int m = 0; m < 2; ++m)
#pragma unroll
    for (int n = 0; n < 5; ++n) acc[m][n] = (f32x4){0.f, 0.f, 0.f, 0.f};
  uint2 rvp[3];
#pragma unroll
  for (int i = 0; i < 3; ++i) {
    int fi = wid + 8 * i; if (fi >= 20) fi = wid;
    rvp[i] = *(const uint2*)(res + (size_t)(bm * BM + (fi / 5) * 16 + fr) * D_ + bn * BN + (fi % 5) * 16 + fq * 4);
  }
#pragma unroll
  for (int i = 0; i < PF; ++i) stage(i);
  wait_vm<(PF - 1) * L>();
  __builtin_amdgcn_s_barrier();
  ldfr(0, 0);
#pragma unroll
  for (int st = 0; st < NT; ++st) {
    const int cur = st & 1;
    if (st + 1 < NT) {
      const int issued = (st + PF - 1 < NT - 1) ? st + PF - 1 : NT - 1;
      const int allow = (issued - (st + 1)) * L;
      if (allow >= L) wait_vm<L>(); else wait_vm<0>();
      __builtin_amdgcn_s_barrier();
      if (st + PF < NT) stage(st + PF);
      ldfr(cur ^ 1, st + 1);
    }
#pragma unroll
    for (int m = 0; m < 2; ++m)
#pragma unroll
      for (int n = 0; n < 5; ++n)
        acc[m][n] = __builtin_amdgcn_mfma_f32_16x16x32_bf16(bfr[cur][n], af[cur][m], acc[m][n], 0, 0, 0);
    if (st + 1 < NT) {
#pragma unroll
      for (int g = 0; g < 5; ++g) {
        __builtin_amdgcn_sched_group_barrier(0x8, 1, 0);
        __builtin_amdgcn_sched_group_barrier(0x10, 1, 0);
        __builtin_amdgcn_sched_group_barrier(0x100, 1, 0);
      }
#pragma unroll
      for (int g = 0; g < 2; ++g) {
        __builtin_amdgcn_sched_group_barrier(0x8, 1, 0);
        __builtin_amdgcn_sched_group_barrier(0x100, 1, 0);
      }
    }
    __builtin_amdgcn_sched_barrier(0);
  }
  __syncthreads();
  f32x4* red = (f32x4*)lds;
#pragma unroll
  for (int m = 0; m < 2; ++m)
#pragma unroll
    for (int n = 0; n < 5; ++n) red[(ks4 * 20 + mh * 10 + m * 5 + n) * 64 + lane] = acc[m][n];
  __syncthreads();
#pragma unroll
  for (int i = 0; i < 3; ++i) {
    int fi = wid + 8 * i;
    if (fi < 20) {
      f32x4 s = red[fi * 64 + lane] + red[(20 + fi) * 64 + lane] + red[(40 + fi) * 64 + lane] + red[(60 + fi) * 64 + lane];
      int mrow = fi / 5, n = fi % 5;
      int row = bm * BM + mrow * 16 + fr;
      int cc = bn * BN + n * 16 + fq * 4;
      uint2 rv = rvp[i];
      float r0 = __uint_as_float(rv.x << 16), r1 = __uint_as_float(rv.x & 0xffff0000u);
      float r2 = __uint_as_float(rv.y << 16), r3 = __uint_as_float(rv.y & 0xffff0000u);
      u16 o0 = f2bf(r0 + rbf(s[0])), o1 = f2bf(r1 + rbf(s[1]));
      u16 o2 = f2bf(r2 + rbf(s[2])), o3 = f2bf(r3 + rbf(s[3]));
      ST2U(out + (size_t)row * D_ + cc, make_uint2(o0 | ((unsigned)o1 << 16), o2 | ((unsigned)o3 << 16)));
    }
  }
}
__global__ __launch_bounds__(512) void k_g2(const u16* __restrict__ A, const u16* __restrict__ Bt, const u16* res, u16* out) {
  __shared__ __attribute__((aligned(16))) char lds[4 * 4 * (64 + 80) * 64];
  const int wid = __builtin_amdgcn_readfirstlane(threadIdx.x >> 6);
  const int xcd = blockIdx.x & 7, idx = blockIdx.x >> 3;
  const int bm = xcd * 4 + (idx & 3), bn = idx >> 2;
  if (wid < 4) g2_body<5>(A, Bt, res, out, lds, bm, bn, wid);
  else g2_body<4>(A, Bt, res, out, lds, bm, bn, wid);
}

extern "C" void kernel_launch(void* const* d_in, const int* in_sizes, int n_in,
                              void* d_out, int out_size, void* d_ws, size_t ws_size,
                              hipStream_t stream) {
  const u16* Ax = (const u16*)d_in[0];
  const u16* wr = (const u16*)d_in[1];
  const u16* wi = (const u16*)d_in[2];
  const u16* w1 = (const u16*)d_in[4];
  const u16* w3 = (const u16*)d_in[5];
  const u16* w2 = (const u16*)d_in[6];
  char* ws = (char*)d_ws;
  size_t off = 0;
  auto alloc = [&](size_t bytes) { void* p = ws + off; off += (bytes + 255) & ~(size_t)255; return p; };
  float2* Zt = (float2*)alloc((size_t)DF_ * S_ * 8);
  unsigned* Wt = (unsigned*)alloc((size_t)DF_ * S_ * 4);
  u16* xb = (u16*)alloc((size_t)S_ * D_ * 2);
  u16* xln = (u16*)alloc((size_t)S_ * D_ * 2);
  u16* h = (u16*)alloc((size_t)S_ * 1792 * 2);
  u16* w13p = (u16*)alloc((size_t)16 * 224 * D_ * 2);
  u16* w2t = (u16*)alloc((size_t)D_ * 1792 * 2);
  u16* outp = (u16*)d_out;

  for (int layer = 0; layer < 2; ++layer) {
    const u16* xin = layer == 0 ? Ax : xb;
    k_a1<<<layer == 0 ? 384 + 160 : 256, 640, 0, stream>>>(xin, Zt, wr, wi, Wt, w1, w3, w2, w13p, w2t);
    k_a2<<<layer == 0 ? DF_ + 160 : DF_, 256, 0, stream>>>(Zt, Wt, w1, w3, w2, w13p, w2t);
    k_a3<<<layer == 0 ? 256 + 160 : 256, 640, 0, stream>>>(Zt, xin, xb, xln, w1, w3, w2, w13p, w2t);
    k_g1<<<256, 512, 0, stream>>>(xln, w13p, h);
    k_g2<<<256, 512, 0, stream>>>(h, w2t, xb, layer == 0 ? xb : outp);
  }
}
```
